```python
import torch

def _softmax(x, axis=-1):
    return torch.softmax(x, dim=axis)

def _gelu(x):
    return torch.nn.functional.gelu(x, approximate='tanh')

def _silu(x):
    return torch.nn.functional.silu(x)

def _sigmoid(x):
    return torch.sigmoid(x)

def _layernorm(x, axis=-1, eps=1e-05):
    m = x.mean(dim=axis, keepdim=True)
    v = x.var(dim=axis, keepdim=True, unbiased=False)
    return (x - m) / torch.sqrt(v + eps)

def _scatter_add(tgt, idx, src, axis=0):
    return tgt.clone().index_add_(axis, idx, src)

def _segment_sum(src, seg, n):
    out = torch.zeros((n,) + src.shape[1:], dtype=src.dtype)
    return out.index_add_(0, seg, src)

def _pad(x, pw):
    flat = tuple((v for pair in reversed(pw) for v in pair))
    return torch.nn.functional.pad(x, flat)
kernel_name = 'adaLN_cond__compressed_kv_cla__glu_gated_geglu__decode'

def setup_inputs():

    _w = lambda a: (lambda t: t.to(torch.bfloat16) if t.is_floating_point() else t)(torch.as_tensor(a))

    def _A_setup():
        B, H, Sq, Sk, D, M = (4, 32, 1, 4096, 128, 640)
        Dc = 4 * D
        return {'x': _w((lambda *s: torch.randn(*s, dtype=torch.bfloat16) * 0.1)(B, Sq, M)), 'c_kv': _w((lambda *s: torch.randn(*s, dtype=torch.bfloat16) * 6.4)(B, Sk, Dc)), 'w_q': _w((lambda *s: torch.randn(*s, dtype=torch.bfloat16) * 0.8)(M, H, D) * M ** (-0.5)), 'w_uk': _w((lambda *s: torch.randn(*s, dtype=torch.bfloat16) * 0.8)(Dc, H, D) * Dc ** (-0.5)), 'w_uv': _w((lambda *s: torch.randn(*s, dtype=torch.bfloat16) * 6.4)(Dc, H, D) * Dc ** (-0.5)), 'w_o': _w((lambda *s: torch.randn(*s, dtype=torch.bfloat16) * 6.4)(H, D, M) * (H * D) ** (-0.5))}
    _A_in = _A_setup()

    def _B_setup():
        B, S, Dm, Df = (4, 1, 640, 1728)
        return {'x': _w((lambda *s: torch.randn(*s, dtype=torch.bfloat16) * 0.1)(B, S, Dm)), 'w_gate': _w((lambda *s: torch.randn(*s, dtype=torch.bfloat16) * 12.8)(Dm, Df) * Dm ** (-0.5)), 'w_up': _w((lambda *s: torch.randn(*s, dtype=torch.bfloat16) * 12.8)(Dm, Df) * Dm ** (-0.5)), 'w_down': _w((lambda *s: torch.randn(*s, dtype=torch.bfloat16) * 12.8)(Df, Dm) * Df ** (-0.5))}
    _B_in = _B_setup()
    return {'A_x': _A_in['x'], 'A_c_kv': _A_in['c_kv'], 'A_w_q': _A_in['w_q'], 'A_w_uk': _A_in['w_uk'], 'A_w_uv': _A_in['w_uv'], 'A_w_o': _A_in['w_o'], 'B_x': _B_in['x'], 'B_w_gate': _B_in['w_gate'], 'B_w_up': _B_in['w_up'], 'B_w_down': _B_in['w_down'], 'c': _w((lambda *s: torch.randn(*s, dtype=torch.bfloat16) * 0.1)(4, 640) * 8.0), 'w_mod': _w((lambda *s: torch.randn(*s, dtype=torch.bfloat16) * 0.1)(640, 6 * 640) * 640 ** (-0.5) * 8.0)}

def reference(A_x, A_c_kv, A_w_q, A_w_uk, A_w_uv, A_w_o, B_x, B_w_gate, B_w_up, B_w_down, c, w_mod):

    def _A_ref(x, c_kv, w_q, w_uk, w_uv, w_o):
        B, Sq, M = (x.shape[0], x.shape[1], x.shape[2])
        Sk = c_kv.shape[1]
        H, D = (w_q.shape[1], w_q.shape[2])
        q = torch.einsum('bqm,mhd->bhqd', x, w_q)
        k = torch.einsum('bkc,chd->bhkd', c_kv, w_uk)
        v = torch.einsum('bkc,chd->bhkd', c_kv, w_uv)
        scores = torch.einsum('bhqd,bhkd->bhqk', q, k) * D ** (-0.5)
        off = Sk - Sq
        qi = torch.arange(Sq)[:, None]
        kj = torch.arange(Sk)[None, :]
        cmask = kj <= qi + off
        scores = torch.where(cmask[None, None], scores, -1000000000.0)
        p = _softmax(scores, axis=-1)
        ctx = torch.einsum('bhqk,bhkd->bhqd', p, v)
        return torch.einsum('bhqd,hdm->bqm', ctx, w_o)

    def _B_ref(x, w_gate, w_up, w_down):
        g = torch.einsum('bsd,df->bsf', x, w_gate)
        u = torch.einsum('bsd,df->bsf', x, w_up)
        h = _gelu(g) * u
        return torch.einsum('bsf,fd->bsd', h, w_down)
    x = A_x
    mod = (_silu(c) @ w_mod).reshape(c.shape[0], 6, 1, -1)
    sa, ca, ga, sb, cb, gb = (mod[:, 0].clone(), mod[:, 1].clone(), mod[:, 2].clone(), mod[:, 3].clone(), mod[:, 4].clone(), mod[:, 5].clone())
    h = _layernorm(x, axis=-1) * (1 + ca) + sa
    x = x + ga * _A_ref(h, A_c_kv, A_w_q, A_w_uk, A_w_uv, A_w_o)
    h = _layernorm(x, axis=-1) * (1 + cb) + sb
    x = x + gb * _B_ref(h, B_w_gate, B_w_up, B_w_down)
    return x
```

```cpp
#include <hip/hip_runtime.h>
#include <hip/hip_bf16.h>
#include <stdint.h>

typedef unsigned short u16;
__device__ __forceinline__ float bf2f(u16 b){ return __uint_as_float(((unsigned)b)<<16); }
__device__ __forceinline__ u16 f2bf(float f){ unsigned u=__float_as_uint(f); u += 0x7FFFu + ((u>>16)&1u); return (u16)(u>>16); }

constexpr int B=4,H=32,SK=4096,D=128,M=640,DC=512,DF=1728;

__global__ void k_mod(const u16* c, const u16* wmod, float* mod){
  __shared__ float s[4][640];
  __shared__ float red[8][4][64];
  int t=threadIdx.x;
  for(int i=t;i<4*640;i+=512){ float v=bf2f(c[i]); s[i/640][i%640]= v/(1.f+__expf(-v)); }
  __syncthreads();
  int col=blockIdx.x*64+(t&63); int w=t>>6;
  float a[4]={0,0,0,0};
  for(int k=w*80;k<w*80+80;k++){ float wv=bf2f(wmod[(size_t)k*3840+col]);
    for(int b=0;b<4;b++) a[b]+=s[b][k]*wv; }
  for(int b=0;b<4;b++) red[w][b][t&63]=a[b];
  __syncthreads();
  if(t<256){ int b=t>>6; float v=0; for(int i=0;i<8;i++) v+=red[i][b][t&63]; mod[b*3840+blockIdx.x*64+(t&63)]=v; }
}
__device__ void ln_mod(const float* xin  , float* hout, const float* mod, int so, int co, float* tmp){
  int t=threadIdx.x; int w=t>>6, l=t&63;
  if(w<4){ float sum=0; for(int i=l;i<640;i+=64) sum+=xin[w*640+i];
    for(int o=32;o;o>>=1) sum+=__shfl_xor(sum,o); float m=sum/640.f; float v=0;
    for(int i=l;i<640;i+=64){ float d=xin[w*640+i]-m; v+=d*d; }
    for(int o=32;o;o>>=1) v+=__shfl_xor(v,o); v/=640.f; float r=rsqrtf(v+1e-5f);
    for(int i=l;i<640;i+=64){ hout[w*640+i]=(xin[w*640+i]-m)*r*(1.f+mod[w*3840+co+i])+mod[w*3840+so+i]; } }
  __syncthreads();
}
__global__ void k_q(const u16* x, const float* mod, const u16* wq, float* q){
  __shared__ float xs[4*640], hs[4*640]; __shared__ float red[8][4][64];
  int t=threadIdx.x;
  for(int i=t;i<2560;i+=512) xs[i]=bf2f(x[i]);
  __syncthreads();
  ln_mod(xs,hs,mod,0,640,nullptr);
  int col=blockIdx.x*64+(t&63); int w=t>>6;
  float a[4]={0,0,0,0};
  for(int k=w*80;k<w*80+80;k++){ float wv=bf2f(wq[(size_t)k*4096+col]); for(int b=0;b<4;b++) a[b]+=hs[b*640+k]*wv; }
  for(int b=0;b<4;b++) red[w][b][t&63]=a[b];
  __syncthreads();
  if(t<256){ int b=t>>6; float v=0; for(int i=0;i<8;i++) v+=red[i][b][t&63]; q[b*4096+blockIdx.x*64+(t&63)]=v; }
}
__global__ void k_qabs(const float* q, const u16* wuk, float* qabs){
  int h=blockIdx.x>>3, cc=blockIdx.x&7; int t=threadIdx.x, w=t>>6, l=t&63;
  for(int ci=0;ci<8;ci++){ int c=cc*64+w*8+ci;
    float w0=bf2f(wuk[((size_t)c*32+h)*128+2*l]), w1=bf2f(wuk[((size_t)c*32+h)*128+2*l+1]);
    for(int b=0;b<4;b++){ float v=q[b*4096+h*128+2*l]*w0+q[b*4096+h*128+2*l+1]*w1;
      for(int o=32;o;o>>=1) v+=__shfl_xor(v,o);
      if(l==0) qabs[(b*32+h)*512+c]=v; } }
}
__global__ void k_attn(const float* qabs, const u16* ckv, float* po, float* pm, float* pl){
  __shared__ float qs[32*512];
  __shared__ float sc[32][64];
  __shared__ float cs[64][512/2];
  int b=blockIdx.x>>6, s=blockIdx.x&63; int t=threadIdx.x, w=t>>6,l=t&63;
  for(int i=t;i<32*512;i+=512) qs[i]=qabs[b*32*512+i];
  __syncthreads();
  const u16* cb=ckv+((size_t)b*4096+s*64)*512;
  for(int ki=0;ki<8;ki++){ int k=w*8+ki; float cv[8]; for(int j=0;j<8;j++) cv[j]=bf2f(cb[k*512+l*8+j]);
    for(int h=0;h<32;h++){ float v=0; for(int j=0;j<8;j++) v+=cv[j]*qs[h*512+l*8+j];
      for(int o=32;o;o>>=1) v+=__shfl_xor(v,o);
      if(l==0) sc[h][k]=v*0.08838834764831845f; } }
  __syncthreads();
  for(int hi=0;hi<4;hi++){ int h=w*4+hi; float v=sc[h][l]; float m=v; for(int o=32;o;o>>=1) m=fmaxf(m,__shfl_xor(m,o));
    float p=__expf(v-m); float su=p; for(int o=32;o;o>>=1) su+=__shfl_xor(su,o); sc[h][l]=p;
    if(l==0){ pm[(b*32+h)*64+s]=m; pl[(b*32+h)*64+s]=su; } }
  __syncthreads();
  float acc[32]; for(int h=0;h<32;h++) acc[h]=0;
  for(int k=0;k<64;k++){ float cv=bf2f(cb[k*512+t]); for(int h=0;h<32;h++) acc[h]+=sc[h][k]*cv; }
  for(int h=0;h<32;h++) po[(((size_t)b*32+h)*64+s)*512+t]=acc[h];
}
__global__ void k_comb(const float* po,const float* pm,const float* pl,const u16* wuv,float* ctx){
  __shared__ float al[64]; __shared__ float cc[512]; __shared__ float red[4][128];
  int bh=blockIdx.x; int h=bh&31; int t=threadIdx.x;
  if(t<64){ float m=pm[bh*64+t]; float mm=m; for(int o=32;o;o>>=1) mm=fmaxf(mm,__shfl_xor(mm,o)); float e=__expf(m-mm); float L=e*pl[bh*64+t]; for(int o=32;o;o>>=1) L+=__shfl_xor(L,o); al[t]=e/L; }
  __syncthreads();
  float v=0; for(int s=0;s<64;s++) v+=al[s]*po[((size_t)bh*64+s)*512+t]; cc[t]=v;
  __syncthreads();
  int d=t&127,g=t>>7; float a=0; for(int c=g*128;c<g*128+128;c++) a+=cc[c]*bf2f(wuv[((size_t)c*32+h)*128+d]); red[g][d]=a;
  __syncthreads();
  if(t<128) ctx[bh*128+t]=red[0][t]+red[1][t]+red[2][t]+red[3][t];
}
__global__ void k_wo(const float* ctx,const u16* wo,const u16* x,const float* mod,float* x1){
  __shared__ float red[32][4][16]; int t=threadIdx.x; int cidx=t&15,g=t>>4; int col=blockIdx.x*16+cidx;
  float a[4]={0,0,0,0};
  for(int k=g*128;k<g*128+128;k++){ float wv=bf2f(wo[(size_t)k*640+col]); for(int b=0;b<4;b++) a[b]+=ctx[b*4096+k]*wv; }
  for(int b=0;b<4;b++) red[g][b][cidx]=a[b];
  __syncthreads();
  if(t<64){ int b=t>>4; float v=0; for(int i=0;i<32;i++) v+=red[i][b][cidx]; x1[b*640+col]=bf2f(x[b*640+col])+mod[b*3840+1280+col]*v; }
}
__global__ void k_gu(const float* x1,const float* mod,const u16* wg,const u16* wu,float* hid){
  __shared__ float xs[2560],hs[2560]; __shared__ float red[32][2][4][16]; int t=threadIdx.x;
  for(int i=t;i<2560;i+=512) xs[i]=x1[i];
  __syncthreads();
  ln_mod(xs,hs,mod,1920,2560,nullptr);
  int cidx=t&15,g=t>>4; int col=blockIdx.x*16+cidx; float a[4]={0,0,0,0},u[4]={0,0,0,0};
  for(int k=g*20;k<g*20+20;k++){ float wv=bf2f(wg[(size_t)k*1728+col]), wv2=bf2f(wu[(size_t)k*1728+col]); for(int b=0;b<4;b++){ a[b]+=hs[b*640+k]*wv; u[b]+=hs[b*640+k]*wv2; } }
  for(int b=0;b<4;b++){ red[g][0][b][cidx]=a[b]; red[g][1][b][cidx]=u[b]; }
  __syncthreads();
  if(t<64){ int b=t>>4; float gg=0,uu=0; for(int i=0;i<32;i++){ gg+=red[i][0][b][cidx]; uu+=red[i][1][b][cidx]; }
    float ge=0.5f*gg*(1.f+tanhf(0.7978845608028654f*(gg+0.044715f*gg*gg*gg))); hid[b*1728+col]=ge*uu; }
}
__global__ void k_down(const float* hid,const u16* wd,const float* x1,const float* mod,u16* out){
  __shared__ float red[32][4][16]; int t=threadIdx.x; int cidx=t&15,g=t>>4; int col=blockIdx.x*16+cidx; float a[4]={0,0,0,0};
  for(int k=g*54;k<g*54+54;k++){ float wv=bf2f(wd[(size_t)k*640+col]); for(int b=0;b<4;b++) a[b]+=hid[b*1728+k]*wv; }
  for(int b=0;b<4;b++) red[g][b][cidx]=a[b];
  __syncthreads();
  if(t<64){ int b=t>>4; float v=0; for(int i=0;i<32;i++) v+=red[i][b][cidx]; out[b*640+col]=f2bf(x1[b*640+col]+mod[b*3840+3200+col]*v); }
}

extern "C" void kernel_launch(void* const* d_in, const int* in_sizes, int n_in,
                              void* d_out, int out_size, void* d_ws, size_t ws_size,
                              hipStream_t stream) {
  const u16* x=(const u16*)d_in[0]; const u16* ckv=(const u16*)d_in[1]; const u16* wq=(const u16*)d_in[2];
  const u16* wuk=(const u16*)d_in[3]; const u16* wuv=(const u16*)d_in[4]; const u16* wo=(const u16*)d_in[5];
  const u16* wg=(const u16*)d_in[7]; const u16* wu=(const u16*)d_in[8]; const u16* wd=(const u16*)d_in[9];
  const u16* c=(const u16*)d_in[10]; const u16* wmod=(const u16*)d_in[11];
  float* ws=(float*)d_ws;
  float* mod=ws; float* q=mod+4*3840; float* qabs=q+4*4096; float* pm=qabs+4*32*512; float* pl=pm+128*64; float* ctx=pl+128*64;
  float* x1=ctx+4*4096; float* hid=x1+2560; float* po=hid+4*1728+1024;
  k_mod<<<60,512,0,stream>>>(c,wmod,mod);
  k_q<<<64,512,0,stream>>>(x,mod,wq,q);
  k_qabs<<<256,512,0,stream>>>(q,wuk,qabs);
  k_attn<<<256,512,0,stream>>>(qabs,ckv,po,pm,pl);
  k_comb<<<128,512,0,stream>>>(po,pm,pl,wuv,ctx);
  k_wo<<<40,512,0,stream>>>(ctx,wo,x,mod,x1);
  k_gu<<<108,512,0,stream>>>(x1,mod,wg,wu,hid);
  k_down<<<40,512,0,stream>>>(hid,wd,x1,mod,(u16*)d_out);
}
```

```cpp
#include <hip/hip_runtime.h>
#include <stdint.h>

namespace {
typedef unsigned short u16;
typedef unsigned u32;
typedef u32 v4u __attribute__((ext_vector_type(4)));
typedef u32 v2u __attribute__((ext_vector_type(2)));
typedef float f32x4 __attribute__((ext_vector_type(4)));
typedef float f32x16 __attribute__((ext_vector_type(16)));
typedef __bf16 bf8 __attribute__((ext_vector_type(8)));
typedef __bf16 bf2 __attribute__((ext_vector_type(2)));
typedef short s16x4 __attribute__((ext_vector_type(4)));
#define GAS __attribute__((address_space(1)))
#define LAS __attribute__((address_space(3)))
#define DEV __device__ __forceinline__

struct Params {
  const u16 *x, *ckv, *wq, *wuk, *wuv, *wo, *wg, *wu, *wd, *c, *wmod;
  u16* out;
  unsigned char* ws;
};

constexpr int OFF_EPOCH = 0;
constexpr int OFF_FLAG1 = 1024;
constexpr int OFF_FLAG2 = 2048;
constexpr int OFF_H = 4096;
constexpr int OFF_Q = 65536;
constexpr int OFF_QABS = 262144;
constexpr int OFF_STATS = 393216;
constexpr int OFF_CTX = 409600;
constexpr int OFF_ATTN = 475136;
constexpr int OFF_MODR = 516096;
constexpr int OFF_HID = 536576;
constexpr int OFF_OUTP = 557056;
constexpr int OFF_O = 2 << 20;
constexpr int WS_USED = OFF_O + (4 << 20);
constexpr int OFF_DBG = 8 << 20;

constexpr u32 SENT = 0xFFFFFFFFu;
constexpr int SPIN_CAP = 40000;
constexpr int AUX_SC1 = 16;
constexpr int LDS_BYTES = 163840;
#define ATT_DELAY 32
#define QDELAY 4

DEV float u2f(u32 u) { return __builtin_bit_cast(float, u); }
DEV u32 f2u(float f) { return __builtin_bit_cast(u32, f); }
DEV float bflo(u32 d) { return u2f(d << 16); }
DEV float bfhi(u32 d) { return u2f(d & 0xFFFF0000u); }
DEV u32 pk2(float lo, float hi) { bf2 v = {(__bf16)lo, (__bf16)hi}; return __builtin_bit_cast(u32, v); }
DEV u32 san(u32 d) { return d < 0xFFFFFFFEu ? d : 0xFFFFFFFEu; }
DEV v4u san4(v4u v) { v.x = san(v.x); v.y = san(v.y); v.z = san(v.z); v.w = san(v.w); return v; }
DEV bool ok4(v4u v) { u32 a = v.x > v.y ? v.x : v.y, b = v.z > v.w ? v.z : v.w; return (a > b ? a : b) != SENT; }
DEV f32x4 mfma16(v4u a, v4u b, f32x4 c) { return __builtin_amdgcn_mfma_f32_16x16x32_bf16(__builtin_bit_cast(bf8, a), __builtin_bit_cast(bf8, b), c, 0, 0, 0); }
DEV f32x16 mfma32(v4u a, v4u b, f32x16 c) { return __builtin_amdgcn_mfma_f32_32x32x16_bf16(__builtin_bit_cast(bf8, a), __builtin_bit_cast(bf8, b), c, 0, 0, 0); }
template <int C> DEV float dppf(float v) { return u2f((u32)__builtin_amdgcn_update_dpp(0, (int)f2u(v), C, 0xf, 0xf, false)); }
DEV float row_sum(float v) { v += dppf<0xB1>(v); v += dppf<0x4E>(v); v += dppf<0x141>(v); v += dppf<0x140>(v); return v; }
DEV float row_max(float v) { v = fmaxf(v, dppf<0xB1>(v)); v = fmaxf(v, dppf<0x4E>(v)); v = fmaxf(v, dppf<0x141>(v)); v = fmaxf(v, dppf<0x140>(v)); return v; }
DEV float x16_sum(float v) { auto r = __builtin_amdgcn_permlane16_swap(f2u(v), f2u(v), false, false); return u2f(r[0]) + u2f(r[1]); }
DEV float x32_sum(float v) { auto r = __builtin_amdgcn_permlane32_swap(f2u(v), f2u(v), false, false); return u2f(r[0]) + u2f(r[1]); }
DEV float x16_max(float v) { auto r = __builtin_amdgcn_permlane16_swap(f2u(v), f2u(v), false, false); return fmaxf(u2f(r[0]), u2f(r[1])); }
DEV float x32_max(float v) { auto r = __builtin_amdgcn_permlane32_swap(f2u(v), f2u(v), false, false); return fmaxf(u2f(r[0]), u2f(r[1])); }
DEV float wave_sum(float v) { return x32_sum(x16_sum(row_sum(v))); }
DEV float silu(float v) { return v / (1.f + __expf(-v)); }

#define BAR() do { asm volatile("s_waitcnt lgkmcnt(0)" ::: "memory"); __builtin_amdgcn_s_barrier(); asm volatile("" ::: "memory"); } while (0)
#define DRAIN() asm volatile("s_waitcnt vmcnt(0)" ::: "memory")
#define LGKM0() asm volatile("s_waitcnt lgkmcnt(0)" ::: "memory")
#define SCHED() __builtin_amdgcn_sched_barrier(0)
#define PIN4(v) asm volatile("" : "+v"(v))

typedef __amdgpu_buffer_rsrc_t rsrc_t;
DEV v4u ld16(rsrc_t rs, int off) { return __builtin_amdgcn_raw_buffer_load_b128(rs, off, 0, AUX_SC1); }
DEV u32 ld4(rsrc_t rs, int off) { return __builtin_amdgcn_raw_buffer_load_b32(rs, off, 0, AUX_SC1); }
DEV void st16(rsrc_t rs, int off, v4u v) { __builtin_amdgcn_raw_buffer_store_b128(v, rs, off, 0, AUX_SC1); }
DEV void st4(rsrc_t rs, int off, u32 v) { __builtin_amdgcn_raw_buffer_store_b32(v, rs, off, 0, AUX_SC1); }
DEV v4u gld16(const u16* p) { return *(const GAS v4u*)p; }
DEV v2u gld8(const u16* p) { return *(const GAS v2u*)p; }
DEV v4u gld16nt(const u16* p) { return __builtin_nontemporal_load((const GAS v4u*)p); }
DEV v2u gld8nt(const u16* p) { return __builtin_nontemporal_load((const GAS v2u*)p); }

DEV void dma16_nt(const void* src, int dst) {
  u32 keep;
  asm volatile("s_mov_b32 %0, m0\n\ts_mov_b32 m0, %2\n\ts_nop 0\n\tglobal_load_lds_dwordx4 %1, off nt\n\ts_mov_b32 m0, %0" : "=&s"(keep) : "v"(src), "s"(dst) : "memory");
}
template <int NP, int RS> DEV f32x4 reduce256(LAS float* red, int w, int lane) {
  const int G = 8 * w + (lane & 7), pg = lane >> 3;
  f32x4 s = *(LAS f32x4*)(red + pg * RS + 4 * G);
#pragma unroll
  for (int k = 1; k < (NP + 7) / 8; ++k) {
    if (8 * k + 8 <= NP) s += *(LAS f32x4*)(red + (pg + 8 * k) * RS + 4 * G);
    else { const bool ok = pg + 8 * k < NP; const f32x4 v = *(LAS f32x4*)(red + (ok ? pg + 8 * k : pg) * RS + 4 * G); s += ok ? v : f32x4{0, 0, 0, 0}; }
  }
#pragma unroll
  for (int c = 0; c < 4; ++c) { float v = s[c]; v += dppf<0x128>(v); v = x32_sum(x16_sum(v)); s[c] = v; }
  return s;
}
template <int NP, int RS> DEV f32x4 reduce256w(LAS float* red, int lane) {
  f32x4 s0 = *(LAS f32x4*)(red + 4 * lane), s1 = *(LAS f32x4*)(red + RS + 4 * lane);
#pragma unroll
  for (int k = 2; k + 1 < NP; k += 2) { s0 += *(LAS f32x4*)(red + k * RS + 4 * lane); s1 += *(LAS f32x4*)(red + (k + 1) * RS + 4 * lane); }
  return s0 + s1;
}
DEV v4u pack8_pairs(f32x4 s) {
  const u32 a = pk2(s[0], s[1]), b = pk2(s[2], s[3]);
  const u32 na = (u32)__builtin_amdgcn_update_dpp(0, (int)a, 0xB1, 0xf, 0xf, false), nb = (u32)__builtin_amdgcn_update_dpp(0, (int)b, 0xB1, 0xf, 0xf, false);
  return v4u{a, b, na, nb};
}
DEV void sleep_n(int n) { for (int i = 0; i < n; ++i) __builtin_amdgcn_s_sleep(1); }

template <int N> DEV void poll16(rsrc_t rs, const int (&off)[N], v4u (&v)[N]) {
  for (int spins = 0;; ++spins) {
    asm volatile("" ::: "memory");
#pragma unroll
    for (int i = 0; i < N; ++i) v[i] = ld16(rs, off[i]);
    bool ok = true;
#pragma unroll
    for (int i = 0; i < N; ++i) ok = ok && ok4(v[i]);
    if (__all(ok) || spins > SPIN_CAP) break;
  }
}
template <int N> DEV void poll16s(rsrc_t rs, const int (&off)[N], v4u (&v)[N], int slp) {
  for (int spins = 0;; ++spins) {
    asm volatile("" ::: "memory");
#pragma unroll
    for (int i = 0; i < N; ++i) v[i] = ld16(rs, off[i]);
    bool ok = true;
#pragma unroll
    for (int i = 0; i < N; ++i) ok = ok && ok4(v[i]);
    if (__all(ok) || spins > SPIN_CAP) break;
    sleep_n(slp);
  }
}
template <int N> DEV void pollg(rsrc_t rs, const int (&off)[N], v4u (&v)[N], u32 tag) {
  for (int spins = 0;; ++spins) {
    asm volatile("" ::: "memory");
#pragma unroll
    for (int i = 0; i < N; ++i) v[i] = ld16(rs, off[i]);
    bool ok = true;
#pragma unroll
    for (int i = 0; i < N; ++i) ok = ok && v[i].y == tag && v[i].w == tag;
    if (__all(ok) || spins > SPIN_CAP) break;
  }
}
DEV void trig4(rsrc_t rs, int off) {
  for (int spins = 0;; ++spins) {
    asm volatile("" ::: "memory");
    const u32 v = ld4(rs, off);
    if (__all(v != SENT) || spins > SPIN_CAP) break;
    __builtin_amdgcn_s_sleep(1);
  }
}
DEV void trigt(rsrc_t rs, int off, u32 tag) {
  for (int spins = 0;; ++spins) {
    asm volatile("" ::: "memory");
    const u32 v = ld4(rs, off);
    if (__all(v == tag) || spins > SPIN_CAP) break;
    __builtin_amdgcn_s_sleep(1);
  }
}
DEV void poll_flags128(rsrc_t rs, int base, int lane, u32 tag) {
  for (int spins = 0;; ++spins) {
    asm volatile("" ::: "memory");
    const v4u f = ld16(rs, base + (lane & 31) * 16);
    if (__all(f.x == tag && f.y == tag && f.z == tag && f.w == tag) || spins > SPIN_CAP) break;
  }
}

#define STAMP(i)
#define STAMP_DECL
#define STAMP_FLUSH()

struct U16 { v4u w[8]; };
struct U8 { v2u w[8]; };
template <bool NT> DEV void load_u16(U16& U, const u16* base, int ld, int lane) {
  const int g = lane >> 4, n = lane & 15;
  const u16* p = base + (size_t)(32 * (n >> 2) + 8 * g) * ld + 8 * (n & 3);
#pragma unroll
  for (int i = 0; i < 8; ++i) U.w[i] = NT ? gld16nt(p + (size_t)i * ld) : gld16(p + (size_t)i * ld);
}
template <bool NT> DEV void load_u16w(U16& U, const u16* base, int ld, int lane) {
  const int g = lane >> 4, n = lane & 15;
  const u16* p = base + (size_t)(32 * (n >> 3) + 8 * g) * ld + 8 * (n & 7);
#pragma unroll
  for (int i = 0; i < 8; ++i) U.w[i] = NT ? gld16nt(p + (size_t)i * ld) : gld16(p + (size_t)i * ld);
}
template <bool NT> DEV void load_u8(U8& U, const u16* base, int ld, int lane) {
  const int g = lane >> 4, n = lane & 15;
  const u16* p = base + (size_t)(32 * (n >> 2) + 8 * g) * ld + 4 * (n & 3);
#pragma unroll
  for (int i = 0; i < 8; ++i) U.w[i] = NT ? gld8nt(p + (size_t)i * ld) : gld8(p + (size_t)i * ld);
}
DEV void tr_u16(const U16& U, v4u (&f)[8]) {
#pragma unroll
  for (int j = 0; j < 8; ++j) {
    const u32 sel = (j & 1) ? 0x07060302u : 0x05040100u;
#pragma unroll
    for (int t = 0; t < 4; ++t) f[j][t] = __builtin_amdgcn_perm(U.w[2 * t + 1][j >> 1], U.w[2 * t][j >> 1], sel);
  }
}
DEV void tr_u8(const U8& U, v4u (&f)[4]) {
#pragma unroll
  for (int j = 0; j < 4; ++j) {
    const u32 sel = (j & 1) ? 0x07060302u : 0x05040100u;
#pragma unroll
    for (int t = 0; t < 4; ++t) f[j][t] = __builtin_amdgcn_perm(U.w[2 * t + 1][j >> 1], U.w[2 * t][j >> 1], sel);
  }
}
DEV v4u silu_frag(v4u cv) {
  v4u o;
#pragma unroll
  for (int t = 0; t < 4; ++t) o[t] = pk2(silu(bflo(cv[t])), silu(bfhi(cv[t])));
  return o;
}

DEV int fsw(int row) { return ((row & 3) << 2) | ((row >> 2) & 3); }
constexpr int RS = 288;
DEV void ch_role(const Params& p, LAS unsigned char* L, rsrc_t rs, int x, int r, int tid, int lane, int w) {
  STAMP_DECL
  STAMP(0);
  const int h = 4 * x + (r >> 2), j = r & 3, dh = j & 1, kh = j >> 1;
  const int chi = 16 * x + r;
  const bool isA = r < 5;
  const int ia = 5 * x + r, slA = ia % 10, matA = (ia / 10) & 1, khA = ia / 20;
  const int g = lane >> 4, n = lane & 15;
  const int e = 14 * x + r;
  const bool isE = r < 14 && e < 108;
  const int bp = j & 1, chf = j >> 1;
  constexpr int UKS = 0;
  constexpr int RED_A = 32768, RED_Q = RED_A + 10 * RS * 4, RED_D = RED_Q + 20 * RS * 4, RED_E = RED_D + 32 * RS * 4, STG = RED_E + 10240, XST = STG + 2048, HS = XST + 128, XE = HS + 5120, QST = XE + 128, HID = QST + 2048, OST = HID + 512;
  static_assert(OST + 6144 <= LDS_BYTES, "lds");
  LAS float* redA = (LAS float*)(L + RED_A);
  LAS float* redQ = (LAS float*)(L + RED_Q);
  LAS float* redE = (LAS float*)(L + RED_E);
  LAS float* redD = (LAS float*)(L + RED_D);
  LAS float* xst = (LAS float*)(L + XST);
  const v4u S4 = {SENT, SENT, SENT, SENT};
  const GAS u32* epp = (const GAS u32*)(p.ws + OFF_EPOCH);

  auto uk_dma = [&](int i0, int i1) {
#pragma unroll
    for (int i = i0; i < i1; ++i) {
      const int row = 4 * i + (lane >> 4);
      const int ch = (lane & 15) ^ fsw(row);
      dma16_nt(p.wuk + ((size_t)(128 * j + row) * 32 + h) * 128 + ch * 8, UKS + 4 * i * 256);
    }
    SCHED();
  };

  U16 uq;
  u32 ep;
  v4u fq[8];
  if (w < 5) {
    if (isA) {
      U16 ua;
      const v4u cfr = gld16(p.c + (n & 3) * 640 + 320 * khA + 64 * w + 32 * ((n >> 2) & 1) + 8 * g);
      load_u16w<true>(ua, p.wmod + (size_t)(320 * khA + 64 * w) * 3840 + 640 * matA + 64 * slA, 3840, lane);
      SCHED();
      const v4u af = silu_frag(cfr);
      v4u fa[8];
      tr_u16(ua, fa);
      STAMP(19);
      f32x4 acc[8];
#pragma unroll
      for (int q = 0; q < 8; ++q) acc[q] = mfma16(af, fa[q], f32x4{0, 0, 0, 0});
      if (g < 2 && (n >> 3) == g) {
#pragma unroll
        for (int b = 0; b < 4; ++b) {
          *(LAS f32x4*)(redA + (w * 2 + g) * RS + b * 64 + 8 * (n & 7)) = f32x4{acc[0][b], acc[1][b], acc[2][b], acc[3][b]};
          *(LAS f32x4*)(redA + (w * 2 + g) * RS + b * 64 + 8 * (n & 7) + 4) = f32x4{acc[4][b], acc[5][b], acc[6][b], acc[7][b]};
        }
      }
      SCHED();
    }
    load_u16w<true>(uq, p.wq + (size_t)(320 * kh + 64 * w) * 4096 + h * 128 + 64 * dh, 4096, lane);
    ep = __hip_atomic_load(epp, __ATOMIC_RELAXED, __HIP_MEMORY_SCOPE_AGENT);
    SCHED();
    STAMP(1);
    if (isA) BAR();
  } else if (w == 7) {
    ep = __hip_atomic_load(epp, __ATOMIC_RELAXED, __HIP_MEMORY_SCOPE_AGENT);
    asm volatile("" ::: "memory");
    st16(rs, OFF_QABS + (lane >> 4) * 32768 + h * 1024 + j * 256 + (lane & 15) * 16, S4);
    if (lane < 32) st16(rs, OFF_CTX + chf * 32768 + (2 * bp + (lane >> 4)) * 8192 + h * 256 + (lane & 15) * 16, S4);
    else if (lane < 40) st16(rs, OFF_STATS + chi * 128 + (lane - 32) * 16, S4);
    if (lane < 32) { if (chi < 80) st16(rs, OFF_ATTN + chi * 512 + lane * 16, S4); }
    else { if (chi < 40) st16(rs, OFF_MODR + chi * 512 + (lane - 32) * 16, S4); }
    if (isE) {
#pragma unroll
      for (int i = 0; i < 5; ++i) st16(rs, OFF_OUTP + ((8 * i + (lane >> 3)) * 108 + e) * 128 + (lane & 7) * 16, S4);
    }
    STAMP(1);
    if (isA) BAR();
    DRAIN();
    if (lane == 0) st4(rs, OFF_FLAG1 + chi * 4, ep + 1u);
    STAMP(2);
    { const int l20 = lane < 20 ? lane : 0; trigt(rs, OFF_H + ((l20 / 5) * 10 + 5 * kh + (l20 % 5)) * 1024 + 1020, ep + 1u); }
  } else if (w == 6) {
    ep = __hip_atomic_load(epp, __ATOMIC_RELAXED, __HIP_MEMORY_SCOPE_AGENT);
    if (isA) {
      u32 d[4][5];
#pragma unroll
      for (int b = 0; b < 4; ++b)
#pragma unroll
        for (int i = 0; i < 5; ++i) d[b][i] = *(const GAS u32*)(p.x + b * 640 + 2 * (lane + 64 * i));
      SCHED();
#pragma unroll
      for (int b = 0; b < 4; ++b) {
        float s = 0.f;
#pragma unroll
        for (int i = 0; i < 5; ++i) s += bflo(d[b][i]) + bfhi(d[b][i]);
        const float m = wave_sum(s) * (1.f / 640.f);
        float q2 = 0.f;
#pragma unroll
        for (int i = 0; i < 5; ++i) { const float d0 = bflo(d[b][i]) - m, d1 = bfhi(d[b][i]) - m; q2 += d0 * d0 + d1 * d1; }
        const float var = wave_sum(q2) * (1.f / 640.f);
        if (lane == 0) { xst[2 * b] = m; xst[2 * b + 1] = rsqrtf(var + 1e-5f); }
      }
      STAMP(1);
      BAR();
    }
    uk_dma(0, 32);
  } else {
    ep = __hip_atomic_load(epp, __ATOMIC_RELAXED, __HIP_MEMORY_SCOPE_AGENT);
    if (isA) {
      const v2u xa2 = gld8(p.x + (lane >> 4) * 640 + 64 * slA + 4 * (lane & 15));
      BAR();
      f32x4 v = reduce256w<10, RS>(redA, lane);
      if (matA) {
        const float m = xst[2 * (lane >> 4)], rstd = xst[2 * (lane >> 4) + 1], one = khA == 0 ? 1.f : 0.f;
        v[0] = (bflo(xa2.x) - m) * rstd * (v[0] + one); v[1] = (bfhi(xa2.x) - m) * rstd * (v[1] + one);
        v[2] = (bflo(xa2.y) - m) * rstd * (v[2] + one); v[3] = (bfhi(xa2.y) - m) * rstd * (v[3] + one);
      }
      st16(rs, OFF_H + ((matA + 2 * khA) * 10 + slA) * 1024 + lane * 16, v4u{pk2(v[0], v[1]), ep + 1u, pk2(v[2], v[3]), ep + 1u});
      STAMP(2);
    }
  }
  const u32 tag = ep + 1u;
  if (w < 5) {
    tr_u16(uq, fq);
#pragma unroll
    for (int q = 0; q < 8; ++q) PIN4(fq[q]);
  }
  STAMP(3);
  BAR();
  STAMP(20);
  if (w < 5) {
    const int o = OFF_H + ((n >> 3) * 10 + 5 * kh + w) * 1024 + (n & 3) * 256 + 128 * ((n >> 2) & 1) + 32 * g;
    int off[4] = {o, o + 16, o + 20480, o + 20480 + 16};
    v4u a[4];
    pollg<4>(rs, off, a, tag);
    STAMP(4);
    const v4u af0 = {a[0].x, a[0].z, a[1].x, a[1].z}, af1 = {a[2].x, a[2].z, a[3].x, a[3].z};
    f32x4 acc[8];
#pragma unroll
    for (int q = 0; q < 8; ++q) { acc[q] = mfma16(af0, fq[q], f32x4{0, 0, 0, 0}); acc[q] = mfma16(af1, fq[q], acc[q]); }
    if ((n >> 3) == (g & 1)) {
#pragma unroll
      for (int b = 0; b < 4; ++b) {
        *(LAS f32x4*)(redQ + (w * 4 + g) * RS + b * 64 + 8 * (n & 7)) = f32x4{acc[0][b], acc[1][b], acc[2][b], acc[3][b]};
        *(LAS f32x4*)(redQ + (w * 4 + g) * RS + b * 64 + 8 * (n & 7) + 4) = f32x4{acc[4][b], acc[5][b], acc[6][b], acc[7][b]};
      }
    }
  }
  BAR();
  STAMP(5);
  if (w == 5) {
    const f32x4 v = reduce256w<20, RS>(redQ, lane);
    st16(rs, OFF_Q + (h * 4 + j) * 1024 + lane * 16, v4u{pk2(v[0], v[1]), tag, pk2(v[2], v[3]), tag});
  }
  STAMP(6);
  {
    if (w < 4) {
      int off[1] = {OFF_Q + h * 4096 + tid * 16}; v4u a[1];
      sleep_n(QDELAY);
      pollg<1>(rs, off, a, tag);
      *(LAS v2u*)(L + QST + tid * 8) = v2u{a[0].x, a[0].z};
    } else if (w == 6) DRAIN();
    BAR();
    STAMP(7);
    f32x4 acc = {0, 0, 0, 0};
    const int row = 16 * w + n;
    v4u af[4], bfr[4];
#pragma unroll
    for (int t = 0; t < 4; ++t) {
      af[t] = *(LAS v4u*)(L + QST + ((((n >> 2) & 1) << 1) + (t >> 1)) * 512 + (n & 3) * 128 + (32 * (t & 1) + 8 * g) * 2);
      bfr[t] = *(LAS v4u*)(L + UKS + row * 256 + (((4 * t + g) ^ fsw(row)) << 4));
    }
#pragma unroll
    for (int t = 0; t < 4; ++t) acc = mfma16(af[t], bfr[t], acc);
    float v;
    {
      const float s0 = x16_sum(acc[0]), s1 = x16_sum(acc[1]), s2 = x16_sum(acc[2]), s3 = x16_sum(acc[3]);
      v = (g == 0 ? s0 : g == 1 ? s1 : g == 2 ? s2 : s3) * (0.08838834764831845f * 1.4426950408889634f);
    }
    LAS u16* stg = (LAS u16*)(L + STG);
    stg[g * 128 + 16 * w + n] = (u16)(pk2(v, 0.f) & 0xFFFFu);
  }
  BAR();
  if (w == 0) {
    const v4u v = *(LAS v4u*)(L + STG + lane * 16);
    st16(rs, OFF_QABS + (lane >> 4) * 32768 + h * 1024 + j * 256 + (lane & 15) * 16, san4(v));
  }
  STAMP(8);
  U16 uv; U8 ug;
  {
    const u16* pv = p.wuv + ((size_t)(256 * chf + 32 * w + 8 * g) * 32 + h) * 128 + 8 * n;
#pragma unroll
    for (int i = 0; i < 8; ++i) uv.w[i] = gld16nt(pv + (size_t)i * 4096);
    if (w < 5 && isE) load_u8<true>(ug, p.wg + (size_t)(128 * w) * 1728 + 16 * e, 1728, lane);
    SCHED();
  }
  STAMP(9);
  if (w == 7) poll_flags128(rs, OFF_FLAG1, lane, tag);
  v4u fv[8];
  tr_u16(uv, fv);
#pragma unroll
  for (int q = 0; q < 8; ++q) PIN4(fv[q]);
  v4u fg[4];
  if (w < 5 && isE) {
    tr_u8(ug, fg);
#pragma unroll
    for (int q = 0; q < 4; ++q) PIN4(fg[q]);
  }
  BAR();
  STAMP(10);
  {
    int off[4]; v4u a[4];
#pragma unroll
    for (int mt = 0; mt < 4; ++mt) off[mt] = OFF_O + (((2 * bp + (mt >> 1)) * 32 + h) * 2 + chf) * 16384 + (16 * (mt & 1) + n) * 512 + 64 * w + 16 * g;
    const int so = OFF_STATS + ((2 * bp + (lane >> 5)) * 32 + (lane & 31)) * 128 + h * 4;
    if (w == 0) trig4(rs, so);
    BAR();
    STAMP(11);
    const u32 lse_u = ld4(rs, so);
#pragma unroll
    for (int mt = 0; mt < 4; ++mt) a[mt] = ld16(rs, off[mt]);
    SCHED();
    const float lse = u2f(lse_u);
    const float mx = x16_max(row_max(lse));
    const float ex = exp2f(lse - mx);
    const float al = ex / x16_sum(row_sum(ex));
    float wt[2][8];
#pragma unroll
    for (int bb = 0; bb < 2; ++bb)
#pragma unroll
      for (int mt = 0; mt < 2; ++mt)
#pragma unroll
        for (int i = 0; i < 4; ++i) wt[bb][mt * 4 + i] = __shfl(al, 32 * bb + 16 * mt + 4 * g + i);
#pragma unroll
    for (int bb = 0; bb < 2; ++bb) {
      f32x4 acc[2][8];
#pragma unroll
      for (int mt = 0; mt < 2; ++mt)
#pragma unroll
        for (int q = 0; q < 8; ++q) acc[mt][q] = mfma16(a[2 * bb + mt], fv[q], f32x4{0, 0, 0, 0});
      if (bb == 0) STAMP(16);
      const f32x4 w0 = {wt[bb][0], wt[bb][1], wt[bb][2], wt[bb][3]}, w1 = {wt[bb][4], wt[bb][5], wt[bb][6], wt[bb][7]};
      float res[8];
#pragma unroll
      for (int q = 0; q < 8; ++q) { const f32x4 t = acc[0][q] * w0 + acc[1][q] * w1; res[q] = (t[0] + t[1]) + (t[2] + t[3]); }
      *(LAS f32x4*)(redD + (w * 4 + g) * RS + bb * 128 + 8 * n) = f32x4{res[0], res[1], res[2], res[3]};
      *(LAS f32x4*)(redD + (w * 4 + g) * RS + bb * 128 + 8 * n + 4) = f32x4{res[4], res[5], res[6], res[7]};
    }
  }
  STAMP(17);
  BAR();
  if (w == 5) {
    const f32x4 v = reduce256w<32, RS>(redD, lane);
    const v4u o = pack8_pairs(v);
    if (!(lane & 1)) st16(rs, OFF_CTX + chf * 32768 + (2 * bp + (lane >> 5)) * 8192 + h * 256 + (lane & 31) * 8, san4(o));
  }
  STAMP(12);
  if (isE) {
    U8 uu; U16 ud;
    if (w < 5) { load_u8<true>(uu, p.wu + (size_t)(128 * w) * 1728 + 16 * e, 1728, lane); SCHED(); }
    else {
      const int col = 256 * (w - 5) + 128 * (g >> 1) + 8 * n;
      const u16* pd = p.wd + (size_t)(16 * e + 8 * (g & 1)) * 640 + (col < 640 ? col : 0);
#pragma unroll
      for (int i = 0; i < 8; ++i) ud.w[i] = gld16nt(pd + (size_t)i * 640);
      SCHED();
    }
    const int eb = w >> 1, ec0 = 320 * (w & 1) + 8 * (lane < 40 ? lane : 39);
    v4u mga, msb, mcb, xe;
    {
      int off[3]; v4u a[3];
#pragma unroll
      for (int k = 0; k < 3; ++k) off[k] = OFF_MODR + (k * 10 + (ec0 >> 6)) * 512 + eb * 128 + (ec0 & 63) * 2;
      xe = gld16(p.x + eb * 640 + ec0);
      poll16<3>(rs, off, a);
      mga = a[0]; msb = a[1]; mcb = a[2];
    }
    v4u fu[4], fd[8];
    if (w < 5) {
      tr_u8(uu, fu);
#pragma unroll
      for (int q = 0; q < 4; ++q) PIN4(fu[q]);
    } else {
      tr_u16(ud, fd);
#pragma unroll
      for (int q = 0; q < 8; ++q) PIN4(fd[q]);
    }
    STAMP(13);
    int off[8]; v4u a[8];
#pragma unroll
    for (int k = 0; k < 8; ++k) off[k] = OFF_ATTN + (k * 10 + (ec0 >> 6)) * 512 + eb * 128 + (ec0 & 63) * 2;
    if (w < 2) trig4(rs, OFF_ATTN + (tid < 80 ? tid : 79) * 512 + 508);
    BAR();
    poll16<8>(rs, off, a);
    STAMP(14);
    float x1[8], s = 0.f;
#pragma unroll
    for (int t = 0; t < 4; ++t) {
      float lo = 0.f, hi = 0.f;
#pragma unroll
      for (int k = 0; k < 8; ++k) { lo += bflo(a[k][t]); hi += bfhi(a[k][t]); }
      x1[2 * t] = bflo(xe[t]) + bflo(mga[t]) * lo;
      x1[2 * t + 1] = bfhi(xe[t]) + bfhi(mga[t]) * hi;
      s += x1[2 * t] + x1[2 * t + 1];
    }
    if (lane >= 40) s = 0.f;
    const float mw = wave_sum(s) * (1.f / 320.f);
    float q2 = 0.f;
#pragma unroll
    for (int i = 0; i < 8; ++i) { const float d = x1[i] - mw; q2 += d * d; }
    if (lane >= 40) q2 = 0.f;
    q2 = wave_sum(q2);
    LAS float* xs = (LAS float*)(L + XE);
    if (lane == 0) { xs[2 * w] = mw; xs[2 * w + 1] = q2; }
    BAR();
    const float mo = xs[2 * (w ^ 1)], qo = xs[2 * (w ^ 1) + 1];
    const float m = 0.5f * (mw + mo);
    const float rstd = rsqrtf((q2 + qo + 160.f * (mw - mo) * (mw - mo)) * (1.f / 640.f) + 1e-5f);
    v4u hv;
#pragma unroll
    for (int t = 0; t < 4; ++t) {
      const float lo = (x1[2 * t] - m) * rstd * (1.f + bflo(mcb[t])) + bflo(msb[t]);
      const float hi = (x1[2 * t + 1] - m) * rstd * (1.f + bfhi(mcb[t])) + bfhi(msb[t]);
      hv[t] = pk2(lo, hi);
    }
    if (lane < 40) *(LAS v4u*)(L + HS + eb * 1280 + ec0 * 2) = hv;
    BAR();
    if (w < 5) {
      const v4u af = *(LAS v4u*)(L + HS + (n & 3) * 1280 + (128 * w + 32 * (n >> 2) + 8 * g) * 2);
      f32x4 ag[4], au[4];
#pragma unroll
      for (int q = 0; q < 4; ++q) { ag[q] = mfma16(af, fg[q], f32x4{0, 0, 0, 0}); au[q] = mfma16(af, fu[q], f32x4{0, 0, 0, 0}); }
      if ((n >> 2) == g) {
#pragma unroll
        for (int b = 0; b < 4; ++b) {
          *(LAS f32x4*)(redE + (w * 4 + g) * 128 + b * 16 + 4 * (n & 3)) = f32x4{ag[0][b], ag[1][b], ag[2][b], ag[3][b]};
          *(LAS f32x4*)(redE + (w * 4 + g) * 128 + 64 + b * 16 + 4 * (n & 3)) = f32x4{au[0][b], au[1][b], au[2][b], au[3][b]};
        }
      }
    }
    BAR();
    STAMP(15);
    if (w >= 5) {
      f32x4 gg = {0, 0, 0, 0}, uu2 = {0, 0, 0, 0};
#pragma unroll
      for (int k = 0; k < 5; ++k) { gg += *(LAS f32x4*)(redE + (4 * k + g) * 128 + 4 * n); uu2 += *(LAS f32x4*)(redE + (4 * k + g) * 128 + 64 + 4 * n); }
      float hd[4];
#pragma unroll
      for (int c = 0; c < 4; ++c) {
        const float gv = x32_sum(x16_sum(gg[c])), uv2 = x32_sum(x16_sum(uu2[c]));
        const float y = 0.7978845608028654f * (gv + 0.044715f * gv * gv * gv);
        const float th = 1.f - 2.f / (__expf(2.f * y) + 1.f);
        hd[c] = 0.5f * gv * (1.f + th) * uv2;
      }
      LAS unsigned char* hb = L + HID + (w - 5) * 128;
      if (g == 0) *(LAS v2u*)(hb + n * 8) = v2u{pk2(hd[0], hd[1]), pk2(hd[2], hd[3])};
      LGKM0();
      v4u af = *(LAS v4u*)(hb + (n & 3) * 32 + (g & 1) * 16);
      if ((g >> 1) != ((n >> 2) & 1)) af = v4u{0, 0, 0, 0};
      f32x4 acc[8];
#pragma unroll
      for (int q = 0; q < 8; ++q) acc[q] = mfma16(af, fd[q], f32x4{0, 0, 0, 0});
      LAS unsigned char* ob = L + OST + (w - 5) * 2048;
      if (g < 2) {
#pragma unroll
        for (int b = 0; b < 4; ++b) {
          v4u o;
#pragma unroll
          for (int t = 0; t < 4; ++t) o[t] = pk2(acc[2 * t][b], acc[2 * t + 1][b]);
          *(LAS v4u*)(ob + (8 * g + (n >> 1)) * 128 + b * 32 + (n & 1) * 16) = san4(o);
        }
      }
      LGKM0();
#pragma unroll
      for (int i = 0; i < 2; ++i) {
        const int ln = 8 * i + (lane >> 3), grp = 16 * (w - 5) + ln;
        const v4u o = *(LAS v4u*)(ob + ln * 128 + (lane & 7) * 16);
        if (grp < 40) st16(rs, OFF_OUTP + (grp * 108 + e) * 128 + (lane & 7) * 16, o);
      }
    }
    STAMP(18);
  }
  STAMP_FLUSH();
}

DEV void att_role(const Params& p, LAS unsigned char* L, rsrc_t rs, int x, int r, int tid, int lane, int w) {
  STAMP_DECL
  STAMP(0);
  const int b = x >> 1, s = ((x & 1) << 4) | r;
  const bool isD3 = r < 10;
  const int id3 = 10 * x + r, ks3 = id3 / 10, slab3 = id3 % 10;
  const bool isM = r >= 10 && r < 15;
  const int slabM = 5 * x + (r - 10);
  const int g = lane >> 4, n = lane & 15, hh = lane >> 5, m32 = lane & 31;
  const int G = 8 * w + (lane & 7);
  constexpr int QS = 131072;
  __builtin_amdgcn_s_sleep(ATT_DELAY);
  {
    const u16* cb = p.ckv + ((size_t)b * 4096 + s * 128 + 16 * w) * 512;
#pragma unroll
    for (int i = 0; i < 16; ++i) {
      const int row = 16 * w + i;
      const int ch = (lane & ~15) | ((lane & 15) ^ fsw(row));
      dma16_nt(cb + (size_t)i * 512 + ch * 8, row * 1024);
    }
  }
  const u32 tag = __hip_atomic_load((GAS u32*)(p.ws + OFF_EPOCH), __ATOMIC_RELAXED, __HIP_MEMORY_SCOPE_AGENT) + 1u;
  STAMP(1);
  if (w == 7) poll_flags128(rs, OFF_FLAG1, lane, tag);
  DRAIN();
  BAR();
  STAMP(2);
  v4u ka[32];
  if (w < 4) {
    const int row = 32 * w + m32;
    const int a0 = row * 1024 + ((hh ^ fsw(row)) << 4);
#pragma unroll
    for (int t = 0; t < 32; ++t) ka[t] = *(LAS v4u*)(L + ((a0 ^ ((t & 7) << 5)) + (t >> 3) * 256));
  }
  v4u pa[2][8];
  const int tq = (lane & 15) >> 2, tp = lane & 3, tcb = g & 1;
  auto read_pa = [&]() {
#pragma unroll
    for (int mt = 0; mt < 2; ++mt)
#pragma unroll
      for (int ks = 0; ks < 8; ++ks)
#pragma unroll
        for (int t2 = 0; t2 < 2; ++t2) {
          const int row = 16 * ks + 8 * t2 + 4 * hh + tq;
          const int ch = 8 * w + 4 * mt + 2 * tcb + (tp >> 1);
          const int f = (tq << 2) | ((2 * t2 + hh) & 3);
          const int addr = row * 1024 + ((ch ^ f) << 4) + 8 * (tp & 1);
          const s16x4 v = __builtin_amdgcn_ds_read_tr16_b64_v4i16((LAS s16x4*)(L + addr));
          const v2u u = __builtin_bit_cast(v2u, v);
          pa[mt][ks][2 * t2] = u.x; pa[mt][ks][2 * t2 + 1] = u.y;
        }
  };
  {
    int off[4]; v4u a[4];
#pragma unroll
    for (int i = 0; i < 4; ++i) off[i] = OFF_QABS + b * 32768 + (tid + 512 * i) * 16;
    if (w >= 4) read_pa();
    STAMP(3);
    if (w >= 6) trig4(rs, OFF_QABS + b * 32768 + (tid - 384) * 256 + 252);
    BAR();
    STAMP(15);
    poll16<4>(rs, off, a);
    STAMP(4);
#pragma unroll
    for (int i = 0; i < 4; ++i) {
      const int head = w + 8 * i, ch = lane;
      *(LAS v4u*)(L + QS + ch * 512 + ((head ^ (ch & 7)) << 4)) = a[i];
    }
  }
  BAR();
  STAMP(5);
  auto scr = [&](int off) { return (off >> 9) * 1024 + 512 + (off & 511); };
  constexpr int SC_P = 0, SC_MX = 8192, SC_LS = 8704;
  f32x16 sacc;
  if (w < 4) {
#pragma unroll
    for (int i = 0; i < 16; ++i) sacc[i] = 0.f;
    auto rdq = [&](int t) { const int ch = 2 * t + hh; return *(LAS v4u*)(L + QS + ch * 512 + ((m32 ^ (ch & 7)) << 4)); };
    v4u qb[8];
#pragma unroll
    for (int t = 0; t < 8; ++t) qb[t] = rdq(t);
    SCHED();
#pragma unroll
    for (int t = 0; t < 32; ++t) {
      const v4u cur = qb[t & 7];
      sacc = mfma32(ka[t], cur, sacc);
      if (t + 8 < 32) qb[t & 7] = rdq(t + 8);
      SCHED();
    }
    float mx = sacc[0];
#pragma unroll
    for (int i = 1; i < 16; ++i) mx = fmaxf(mx, sacc[i]);
    mx = x32_max(mx);
    if (hh == 0) *(LAS float*)(L + scr(SC_MX + (w * 32 + m32) * 4)) = mx;
    STAMP(6);
  }
  BAR();
  STAMP(7);
  float M;
  {
    float m0 = *(LAS float*)(L + scr(SC_MX + (0 * 32 + m32) * 4)), m1 = *(LAS float*)(L + scr(SC_MX + (1 * 32 + m32) * 4));
    float m2 = *(LAS float*)(L + scr(SC_MX + (2 * 32 + m32) * 4)), m3 = *(LAS float*)(L + scr(SC_MX + (3 * 32 + m32) * 4));
    M = fmaxf(fmaxf(m0, m1), fmaxf(m2, m3));
  }
  if (w < 4) {
    float pr[16], ls = 0.f;
#pragma unroll
    for (int i = 0; i < 16; ++i) { pr[i] = __builtin_amdgcn_exp2f(sacc[i] - M); ls += pr[i]; }
    ls = x32_sum(ls);
    if (hh == 0) *(LAS float*)(L + scr(SC_LS + (w * 32 + m32) * 4)) = ls;
#pragma unroll
    for (int sp = 0; sp < 2; ++sp) {
      v4u pf;
#pragma unroll
      for (int t = 0; t < 4; ++t) pf[t] = pk2(pr[8 * sp + 2 * t], pr[8 * sp + 2 * t + 1]);
      *(LAS v4u*)(L + scr(SC_P + ((2 * w + sp) * 64 + lane) * 16)) = pf;
    }
    read_pa();
  }
  BAR();
  STAMP(8);
  float lsum;
  {
    float l0 = *(LAS float*)(L + scr(SC_LS + (0 * 32 + m32) * 4)), l1 = *(LAS float*)(L + scr(SC_LS + (1 * 32 + m32) * 4));
    float l2 = *(LAS float*)(L + scr(SC_LS + (2 * 32 + m32) * 4)), l3 = *(LAS float*)(L + scr(SC_LS + (3 * 32 + m32) * 4));
    lsum = (l0 + l1) + (l2 + l3);
  }
  f32x16 oacc[2];
#pragma unroll
  for (int mt = 0; mt < 2; ++mt)
#pragma unroll
    for (int i = 0; i < 16; ++i) oacc[mt][i] = 0.f;
  {
    v4u pf[8];
#pragma unroll
    for (int ks = 0; ks < 8; ++ks) pf[ks] = *(LAS v4u*)(L + scr(SC_P + (ks * 64 + lane) * 16));
    SCHED();
#pragma unroll
    for (int ks = 0; ks < 8; ++ks) {
      oacc[0] = mfma32(pa[0][ks], pf[ks], oacc[0]);
      oacc[1] = mfma32(pa[1][ks], pf[ks], oacc[1]);
    }
  }
  STAMP(9);
  {
    const float inv = 1.f / lsum;
    LAS unsigned char* st = L + QS + w * 4096;
#pragma unroll
    for (int mt = 0; mt < 2; ++mt)
#pragma unroll
      for (int qd = 0; qd < 4; ++qd) {
        v2u o;
        o.x = pk2(oacc[mt][4 * qd] * inv, oacc[mt][4 * qd + 1] * inv);
        o.y = pk2(oacc[mt][4 * qd + 2] * inv, oacc[mt][4 * qd + 3] * inv);
        *(LAS v2u*)(st + m32 * 128 + (((4 * mt + qd) ^ (m32 & 7)) << 4) + 8 * hh) = o;
      }
    LGKM0();
#pragma unroll
    for (int i = 0; i < 4; ++i) {
      const int head = 8 * i + (lane >> 3), ch = lane & 7;
      const v4u v = *(LAS v4u*)(st + head * 128 + ((ch ^ (head & 7)) << 4));
      st16(rs, OFF_O + ((b * 32 + head) * 2 + (w >> 2)) * 16384 + s * 512 + (w & 3) * 128 + ch * 16, v);
    }
    STAMP(16);
    DRAIN();
    BAR();
    if (w == 0) {
      const float lse = M + __log2f(lsum);
      LAS float* sf = (LAS float*)(L + scr(SC_MX + 1024));
      if (hh == 0) sf[m32] = lse;
      LGKM0();
      if (lane < 8) { const v4u v = *(LAS v4u*)(sf + 4 * lane); st16(rs, OFF_STATS + (b * 32 + s) * 128 + lane * 16, san4(v)); }
    }
  }
  STAMP(10);
  constexpr int RED_3 = 32768, RED_F = 98304;
  if (isD3) {
    U16 uo;
    load_u16w<true>(uo, p.wo + (size_t)(512 * ks3 + 64 * w) * 640 + 64 * slab3, 640, lane);
    SCHED();
    v4u fo[8];
    tr_u16(uo, fo);
#pragma unroll
    for (int q = 0; q < 8; ++q) PIN4(fo[q]);
    STAMP(11);
    const int k0 = 512 * ks3 + 64 * w + 32 * ((n >> 2) & 1) + 8 * g;
    int off[1] = {OFF_CTX + (n >> 3) * 32768 + (n & 3) * 8192 + k0 * 2};
    v4u a[1];
    if (w == 0) trig4(rs, OFF_CTX + ((lane >> 2) & 1) * 32768 + ((lane >> 3) & 3) * 8192 + (4 * ks3 + (lane & 3)) * 256 + 252);
    if (w == 0 && id3 == 0 && lane == 0) st4(rs, OFF_EPOCH, tag);
    BAR();
    poll16<1>(rs, off, a);
    STAMP(12);
    f32x4 acc[8];
#pragma unroll
    for (int q = 0; q < 8; ++q) acc[q] = mfma16(a[0], fo[q], f32x4{0, 0, 0, 0});
    LAS float* red = (LAS float*)(L + RED_3);
    if ((n >> 3) == (g & 1)) {
#pragma unroll
      for (int bb = 0; bb < 4; ++bb) {
        *(LAS f32x4*)(red + (w * 4 + g) * RS + bb * 64 + 8 * (n & 7)) = f32x4{acc[0][bb], acc[1][bb], acc[2][bb], acc[3][bb]};
        *(LAS f32x4*)(red + (w * 4 + g) * RS + bb * 64 + 8 * (n & 7) + 4) = f32x4{acc[4][bb], acc[5][bb], acc[6][bb], acc[7][bb]};
      }
    }
    BAR();
    if (w == 0) {
      const f32x4 v = reduce256w<32, RS>(red, lane);
      const v4u o = pack8_pairs(v);
      if (!(lane & 1)) st16(rs, OFF_ATTN + id3 * 512 + lane * 8, san4(o));
    }
    STAMP(13);
  }
  if (isM) {
    U16 um[2]; v4u cf[2];
    LAS float* red = (LAS float*)(L + RED_3);
    if (w < 5) {
#pragma unroll
      for (int u = 0; u < 2; ++u) {
        cf[u] = gld16(p.c + (n & 3) * 640 + 128 * w + 64 * u + 32 * ((n >> 2) & 1) + 8 * g);
        load_u16w<true>(um[u], p.wmod + (size_t)(128 * w + 64 * u) * 3840 + 1280 + 64 * slabM, 3840, lane);
      }
      SCHED();
      f32x4 acc[8];
#pragma unroll
      for (int q = 0; q < 8; ++q) acc[q] = f32x4{0, 0, 0, 0};
#pragma unroll
      for (int u = 0; u < 2; ++u) {
        v4u fm[8];
        tr_u16(um[u], fm);
        const v4u af = silu_frag(cf[u]);
#pragma unroll
        for (int q = 0; q < 8; ++q) acc[q] = mfma16(af, fm[q], acc[q]);
      }
      if (g < 2 && (n >> 3) == g) {
#pragma unroll
        for (int bb = 0; bb < 4; ++bb) {
          *(LAS f32x4*)(red + (w * 2 + g) * RS + bb * 64 + 8 * (n & 7)) = f32x4{acc[0][bb], acc[1][bb], acc[2][bb], acc[3][bb]};
          *(LAS f32x4*)(red + (w * 2 + g) * RS + bb * 64 + 8 * (n & 7) + 4) = f32x4{acc[4][bb], acc[5][bb], acc[6][bb], acc[7][bb]};
        }
      }
    }
    BAR();
    if (w == 1) {
      const f32x4 v = reduce256w<10, RS>(red, lane);
      const v4u o = pack8_pairs(v);
      if (!(lane & 1)) st16(rs, OFF_MODR + slabM * 512 + lane * 8, san4(o));
    }
    STAMP(11);
    float x1 = 0.f, gbv = 0.f;
    if (w == 0) {
      const int bb = lane >> 4, col = 16 * slabM + (lane & 15);
      const int sub = bb * 128 + ((col & 63) * 2 & ~3);
      const u32 xv = (u32)p.x[bb * 640 + col];
      u32 v[10];
      for (int spins = 0;; ++spins) {
        asm volatile("" ::: "memory");
#pragma unroll
        for (int k = 0; k < 8; ++k) v[k] = ld4(rs, OFF_ATTN + (k * 10 + (col >> 6)) * 512 + sub);
        v[8] = ld4(rs, OFF_MODR + (0 * 10 + (col >> 6)) * 512 + sub);
        v[9] = ld4(rs, OFF_MODR + (3 * 10 + (col >> 6)) * 512 + sub);
        bool ok = true;
#pragma unroll
        for (int k = 0; k < 10; ++k) ok = ok && v[k] != SENT;
        if (__all(ok) || spins > SPIN_CAP) break;
        __builtin_amdgcn_s_sleep(4);
      }
      float f[10], at = 0.f;
#pragma unroll
      for (int k = 0; k < 10; ++k) f[k] = (col & 1) ? bfhi(v[k]) : bflo(v[k]);
#pragma unroll
      for (int k = 0; k < 8; ++k) at += f[k];
      x1 = bflo(xv) + f[8] * at;
      gbv = f[9];
    }
    BAR();
    STAMP(12);
    const int e0 = tid >> 3, e1 = (64 + e0 < 108) ? 64 + e0 : e0;
    int off[2] = {OFF_OUTP + (slabM * 108 + e0) * 128 + (tid & 7) * 16, OFF_OUTP + (slabM * 108 + e1) * 128 + (tid & 7) * 16};
    v4u a[2];
    if (w >= 6) { const int t2 = tid - 384; trig4(rs, OFF_OUTP + (slabM * 108 + (t2 < 108 ? t2 : 107)) * 128 + 124); }
    BAR();
    poll16<2>(rs, off, a);
    STAMP(13);
    float v8[8];
    const float m1 = (64 + e0 < 108) ? 1.f : 0.f;
#pragma unroll
    for (int t = 0; t < 4; ++t) { v8[2 * t] = bflo(a[0][t]) + m1 * bflo(a[1][t]); v8[2 * t + 1] = bfhi(a[0][t]) + m1 * bfhi(a[1][t]); }
#pragma unroll
    for (int t = 0; t < 8; ++t) { float v = v8[t]; v += dppf<0x128>(v); v = x32_sum(x16_sum(v)); v8[t] = v; }
    LAS float* redf = (LAS float*)(L + RED_F);
    if (lane < 8) {
      *(LAS f32x4*)(redf + w * 64 + lane * 8) = f32x4{v8[0], v8[1], v8[2], v8[3]};
      *(LAS f32x4*)(redf + w * 64 + lane * 8 + 4) = f32x4{v8[4], v8[5], v8[6], v8[7]};
    }
    BAR();
    if (w == 0) {
      float y = 0.f;
#pragma unroll
      for (int i = 0; i < 8; ++i) y += redf[i * 64 + lane];
      const float o = x1 + gbv * y;
      p.out[(lane >> 4) * 640 + 16 * slabM + (lane & 15)] = (u16)(pk2(o, 0.f) & 0xFFFFu);
    }
    STAMP(17);
  }
  STAMP_FLUSH();
}

__global__ void __launch_bounds__(512, 2) mk_kernel(Params p) {
  extern __shared__ __attribute__((aligned(16))) unsigned char smem[];
  LAS unsigned char* L = (LAS unsigned char*)smem;
  const int tid = threadIdx.x, lane = tid & 63;
  const int w = __builtin_amdgcn_readfirstlane(tid >> 6);
  const int x = blockIdx.x & 7, r = blockIdx.x >> 3;
  const rsrc_t rs = __builtin_amdgcn_make_buffer_rsrc((void*)p.ws, 0, WS_USED, 0x00020000);
  if (r < 16) ch_role(p, L, rs, x, r, tid, lane, w);
  else att_role(p, L, rs, x, r - 16, tid, lane, w);
}
}

extern "C" void kernel_launch(void* const* d_in, const int* in_sizes, int n_in, void* d_out, int out_size, void* d_ws, size_t ws_size, hipStream_t stream) {
  Params p;
  p.x = (const u16*)d_in[0]; p.ckv = (const u16*)d_in[1]; p.wq = (const u16*)d_in[2]; p.wuk = (const u16*)d_in[3];
  p.wuv = (const u16*)d_in[4]; p.wo = (const u16*)d_in[5]; p.wg = (const u16*)d_in[7]; p.wu = (const u16*)d_in[8];
  p.wd = (const u16*)d_in[9]; p.c = (const u16*)d_in[10]; p.wmod = (const u16*)d_in[11];
  p.out = (u16*)d_out; p.ws = (unsigned char*)d_ws;
  (void)hipFuncSetAttribute((const void*)mk_kernel, hipFuncAttributeMaxDynamicSharedMemorySize, LDS_BYTES);
  hipLaunchKernelGGL(mk_kernel, dim3(256), dim3(512), LDS_BYTES, stream, p);
}
```

```cpp
#include <hip/hip_runtime.h>
#include <stdint.h>

namespace {
typedef unsigned short u16;
typedef unsigned u32;
typedef u32 v4u __attribute__((ext_vector_type(4)));
typedef u32 v2u __attribute__((ext_vector_type(2)));
typedef float f32x4 __attribute__((ext_vector_type(4)));
typedef float f32x16 __attribute__((ext_vector_type(16)));
typedef __bf16 bf8 __attribute__((ext_vector_type(8)));
typedef __bf16 bf2 __attribute__((ext_vector_type(2)));
typedef short s16x4 __attribute__((ext_vector_type(4)));
#define GAS __attribute__((address_space(1)))
#define LAS __attribute__((address_space(3)))
#define DEV __device__ __forceinline__

struct Params {
  const u16 *x, *ckv, *wq, *wuk, *wuv, *wo, *wg, *wu, *wd, *c, *wmod;
  u16* out;
  unsigned char* ws;
};

constexpr int OFF_EPOCH = 0;
constexpr int OFF_FLAG1 = 1024;
constexpr int OFF_FLAG2 = 2048;
constexpr int OFF_H = 4096;
constexpr int OFF_Q = 65536;
constexpr int OFF_QABS = 262144;
constexpr int OFF_STATS = 393216;
constexpr int OFF_CTX = 409600;
constexpr int OFF_ATTN = 475136;
constexpr int OFF_MODR = 516096;
constexpr int OFF_HID = 536576;
constexpr int OFF_OUTP = 557056;
constexpr int OFF_O = 2 << 20;
constexpr int WS_USED = OFF_O + (4 << 20);
constexpr int OFF_DBG = 8 << 20;

constexpr u32 SENT = 0xFFFFFFFFu;
constexpr int SPIN_CAP = 40000;
constexpr int AUX_SC1 = 16;
constexpr int LDS_BYTES = 163840;
#define CODE_PREFETCH 1
constexpr int CODE_BYTES = 34000;
#define ATT_DELAY 32
#define QDELAY 4

DEV float u2f(u32 u) { return __builtin_bit_cast(float, u); }
DEV u32 f2u(float f) { return __builtin_bit_cast(u32, f); }
DEV float bflo(u32 d) { return u2f(d << 16); }
DEV float bfhi(u32 d) { return u2f(d & 0xFFFF0000u); }
DEV u32 pk2(float lo, float hi) { bf2 v = {(__bf16)lo, (__bf16)hi}; return __builtin_bit_cast(u32, v); }
DEV u32 san(u32 d) { return d < 0xFFFFFFFEu ? d : 0xFFFFFFFEu; }
DEV v4u san4(v4u v) { v.x = san(v.x); v.y = san(v.y); v.z = san(v.z); v.w = san(v.w); return v; }
DEV bool ok4(v4u v) { u32 a = v.x > v.y ? v.x : v.y, b = v.z > v.w ? v.z : v.w; return (a > b ? a : b) != SENT; }
DEV f32x4 mfma16(v4u a, v4u b, f32x4 c) { return __builtin_amdgcn_mfma_f32_16x16x32_bf16(__builtin_bit_cast(bf8, a), __builtin_bit_cast(bf8, b), c, 0, 0, 0); }
DEV f32x16 mfma32(v4u a, v4u b, f32x16 c) { return __builtin_amdgcn_mfma_f32_32x32x16_bf16(__builtin_bit_cast(bf8, a), __builtin_bit_cast(bf8, b), c, 0, 0, 0); }
template <int C> DEV float dppf(float v) { return u2f((u32)__builtin_amdgcn_update_dpp(0, (int)f2u(v), C, 0xf, 0xf, false)); }
DEV float row_sum(float v) { v += dppf<0xB1>(v); v += dppf<0x4E>(v); v += dppf<0x141>(v); v += dppf<0x140>(v); return v; }
DEV float row_max(float v) { v = fmaxf(v, dppf<0xB1>(v)); v = fmaxf(v, dppf<0x4E>(v)); v = fmaxf(v, dppf<0x141>(v)); v = fmaxf(v, dppf<0x140>(v)); return v; }
DEV float x16_sum(float v) { auto r = __builtin_amdgcn_permlane16_swap(f2u(v), f2u(v), false, false); return u2f(r[0]) + u2f(r[1]); }
DEV float x32_sum(float v) { auto r = __builtin_amdgcn_permlane32_swap(f2u(v), f2u(v), false, false); return u2f(r[0]) + u2f(r[1]); }
DEV float x16_max(float v) { auto r = __builtin_amdgcn_permlane16_swap(f2u(v), f2u(v), false, false); return fmaxf(u2f(r[0]), u2f(r[1])); }
DEV float x32_max(float v) { auto r = __builtin_amdgcn_permlane32_swap(f2u(v), f2u(v), false, false); return fmaxf(u2f(r[0]), u2f(r[1])); }
DEV float wave_sum(float v) { return x32_sum(x16_sum(row_sum(v))); }
DEV float silu(float v) { return v / (1.f + __expf(-v)); }

#define BAR() do { asm volatile("s_waitcnt lgkmcnt(0)" ::: "memory"); __builtin_amdgcn_s_barrier(); asm volatile("" ::: "memory"); } while (0)
#define DRAIN() asm volatile("s_waitcnt vmcnt(0)" ::: "memory")
#define LGKM0() asm volatile("s_waitcnt lgkmcnt(0)" ::: "memory")
#define SCHED() __builtin_amdgcn_sched_barrier(0)
#define PIN4(v) asm volatile("" : "+v"(v))

typedef __amdgpu_buffer_rsrc_t rsrc_t;
DEV v4u ld16(rsrc_t rs, int off) { return __builtin_amdgcn_raw_buffer_load_b128(rs, off, 0, AUX_SC1); }
DEV u32 ld4(rsrc_t rs, int off) { return __builtin_amdgcn_raw_buffer_load_b32(rs, off, 0, AUX_SC1); }
DEV void st16(rsrc_t rs, int off, v4u v) { __builtin_amdgcn_raw_buffer_store_b128(v, rs, off, 0, AUX_SC1); }
DEV void st4(rsrc_t rs, int off, u32 v) { __builtin_amdgcn_raw_buffer_store_b32(v, rs, off, 0, AUX_SC1); }
DEV v4u gld16(const u16* p) { return *(const GAS v4u*)p; }
DEV v2u gld8(const u16* p) { return *(const GAS v2u*)p; }
DEV v4u gld16nt(const u16* p) { return __builtin_nontemporal_load((const GAS v4u*)p); }
DEV v2u gld8nt(const u16* p) { return __builtin_nontemporal_load((const GAS v2u*)p); }

DEV void dma16_nt(const void* src, int dst) {
  u32 keep;
  asm volatile("s_mov_b32 %0, m0\n\ts_mov_b32 m0, %2\n\ts_nop 0\n\tglobal_load_lds_dwordx4 %1, off nt\n\ts_mov_b32 m0, %0" : "=&s"(keep) : "v"(src), "s"(dst) : "memory");
}
template <int NP, int RS> DEV f32x4 reduce256(LAS float* red, int w, int lane) {
  const int G = 8 * w + (lane & 7), pg = lane >> 3;
  f32x4 s = *(LAS f32x4*)(red + pg * RS + 4 * G);
#pragma unroll
  for (int k = 1; k < (NP + 7) / 8; ++k) {
    if (8 * k + 8 <= NP) s += *(LAS f32x4*)(red + (pg + 8 * k) * RS + 4 * G);
    else { const bool ok = pg + 8 * k < NP; const f32x4 v = *(LAS f32x4*)(red + (ok ? pg + 8 * k : pg) * RS + 4 * G); s += ok ? v : f32x4{0, 0, 0, 0}; }
  }
#pragma unroll
  for (int c = 0; c < 4; ++c) { float v = s[c]; v += dppf<0x128>(v); v = x32_sum(x16_sum(v)); s[c] = v; }
  return s;
}
template <int NP, int RS> DEV f32x4 reduce256w(LAS float* red, int lane) {
  f32x4 s0 = *(LAS f32x4*)(red + 4 * lane), s1 = *(LAS f32x4*)(red + RS + 4 * lane);
#pragma unroll
  for (int k = 2; k + 1 < NP; k += 2) { s0 += *(LAS f32x4*)(red + k * RS + 4 * lane); s1 += *(LAS f32x4*)(red + (k + 1) * RS + 4 * lane); }
  return s0 + s1;
}
DEV v4u pack8_pairs(f32x4 s) {
  const u32 a = pk2(s[0], s[1]), b = pk2(s[2], s[3]);
  const u32 na = (u32)__builtin_amdgcn_update_dpp(0, (int)a, 0xB1, 0xf, 0xf, false), nb = (u32)__builtin_amdgcn_update_dpp(0, (int)b, 0xB1, 0xf, 0xf, false);
  return v4u{a, b, na, nb};
}
DEV void sleep_n(int n) { for (int i = 0; i < n; ++i) __builtin_amdgcn_s_sleep(1); }

template <int N> DEV void poll16(rsrc_t rs, const int (&off)[N], v4u (&v)[N]) {
  for (int spins = 0;; ++spins) {
    asm volatile("" ::: "memory");
#pragma unroll
    for (int i = 0; i < N; ++i) v[i] = ld16(rs, off[i]);
    bool ok = true;
#pragma unroll
    for (int i = 0; i < N; ++i) ok = ok && ok4(v[i]);
    if (__all(ok) || spins > SPIN_CAP) break;
  }
}
template <int N> DEV void poll16s(rsrc_t rs, const int (&off)[N], v4u (&v)[N], int slp) {
  for (int spins = 0;; ++spins) {
    asm volatile("" ::: "memory");
#pragma unroll
    for (int i = 0; i < N; ++i) v[i] = ld16(rs, off[i]);
    bool ok = true;
#pragma unroll
    for (int i = 0; i < N; ++i) ok = ok && ok4(v[i]);
    if (__all(ok) || spins > SPIN_CAP) break;
    sleep_n(slp);
  }
}
template <int N> DEV void pollg(rsrc_t rs, const int (&off)[N], v4u (&v)[N], u32 tag) {
  for (int spins = 0;; ++spins) {
    asm volatile("" ::: "memory");
#pragma unroll
    for (int i = 0; i < N; ++i) v[i] = ld16(rs, off[i]);
    bool ok = true;
#pragma unroll
    for (int i = 0; i < N; ++i) ok = ok && v[i].y == tag && v[i].w == tag;
    if (__all(ok) || spins > SPIN_CAP) break;
  }
}
DEV void trig4(rsrc_t rs, int off) {
  for (int spins = 0;; ++spins) {
    asm volatile("" ::: "memory");
    const u32 v = ld4(rs, off);
    if (__all(v != SENT) || spins > SPIN_CAP) break;
    __builtin_amdgcn_s_sleep(1);
  }
}
DEV void trigt(rsrc_t rs, int off, u32 tag) {
  for (int spins = 0;; ++spins) {
    asm volatile("" ::: "memory");
    const u32 v = ld4(rs, off);
    if (__all(v == tag) || spins > SPIN_CAP) break;
    __builtin_amdgcn_s_sleep(1);
  }
}
DEV void poll_flags128(rsrc_t rs, int base, int lane, u32 tag) {
  for (int spins = 0;; ++spins) {
    asm volatile("" ::: "memory");
    const v4u f = ld16(rs, base + (lane & 31) * 16);
    if (__all(f.x == tag && f.y == tag && f.z == tag && f.w == tag) || spins > SPIN_CAP) break;
  }
}

#define STAMP(i)
#define STAMP_DECL
#define STAMP_FLUSH()

struct U16 { v4u w[8]; };
struct U8 { v2u w[8]; };
template <bool NT> DEV void load_u16(U16& U, const u16* base, int ld, int lane) {
  const int g = lane >> 4, n = lane & 15;
  const u16* p = base + (size_t)(32 * (n >> 2) + 8 * g) * ld + 8 * (n & 3);
#pragma unroll
  for (int i = 0; i < 8; ++i) U.w[i] = NT ? gld16nt(p + (size_t)i * ld) : gld16(p + (size_t)i * ld);
}
template <bool NT> DEV void load_u16w(U16& U, const u16* base, int ld, int lane) {
  const int g = lane >> 4, n = lane & 15;
  const u16* p = base + (size_t)(32 * (n >> 3) + 8 * g) * ld + 8 * (n & 7);
#pragma unroll
  for (int i = 0; i < 8; ++i) U.w[i] = NT ? gld16nt(p + (size_t)i * ld) : gld16(p + (size_t)i * ld);
}
template <bool NT> DEV void load_u8(U8& U, const u16* base, int ld, int lane) {
  const int g = lane >> 4, n = lane & 15;
  const u16* p = base + (size_t)(32 * (n >> 2) + 8 * g) * ld + 4 * (n & 3);
#pragma unroll
  for (int i = 0; i < 8; ++i) U.w[i] = NT ? gld8nt(p + (size_t)i * ld) : gld8(p + (size_t)i * ld);
}
DEV void tr_u16(const U16& U, v4u (&f)[8]) {
#pragma unroll
  for (int j = 0; j < 8; ++j) {
    const u32 sel = (j & 1) ? 0x07060302u : 0x05040100u;
#pragma unroll
    for (int t = 0; t < 4; ++t) f[j][t] = __builtin_amdgcn_perm(U.w[2 * t + 1][j >> 1], U.w[2 * t][j >> 1], sel);
  }
}
DEV void tr_u8(const U8& U, v4u (&f)[4]) {
#pragma unroll
  for (int j = 0; j < 4; ++j) {
    const u32 sel = (j & 1) ? 0x07060302u : 0x05040100u;
#pragma unroll
    for (int t = 0; t < 4; ++t) f[j][t] = __builtin_amdgcn_perm(U.w[2 * t + 1][j >> 1], U.w[2 * t][j >> 1], sel);
  }
}
DEV v4u silu_frag(v4u cv) {
  v4u o;
#pragma unroll
  for (int t = 0; t < 4; ++t) o[t] = pk2(silu(bflo(cv[t])), silu(bfhi(cv[t])));
  return o;
}

DEV int fsw(int row) { return ((row & 3) << 2) | ((row >> 2) & 3); }
constexpr int RS = 288;
DEV void ch_role(const Params& p, LAS unsigned char* L, rsrc_t rs, int x, int r, int tid, int lane, int w) {
  STAMP_DECL
  STAMP(0);
  const int h = 4 * x + (r >> 2), j = r & 3, dh = j & 1, kh = j >> 1;
  const int chi = 16 * x + r;
  const bool isA = r < 5;
  const int ia = 5 * x + r, slA = ia % 10, matA = (ia / 10) & 1, khA = ia / 20;
  const int g = lane >> 4, n = lane & 15;
  const int e = 14 * x + r;
  const bool isE = r < 14 && e < 108;
  const int bp = j & 1, chf = j >> 1;
  constexpr int UKS = 0;
  constexpr int RED_A = 32768, RED_Q = RED_A + 10 * RS * 4, RED_D = RED_Q + 20 * RS * 4, RED_E = RED_D + 32 * RS * 4, STG = RED_E + 10240, XST = STG + 2048, HS = XST + 128, XE = HS + 5120, QST = XE + 128, HID = QST + 2048, OST = HID + 512;
  static_assert(OST + 6144 <= LDS_BYTES, "lds");
  LAS float* redA = (LAS float*)(L + RED_A);
  LAS float* redQ = (LAS float*)(L + RED_Q);
  LAS float* redE = (LAS float*)(L + RED_E);
  LAS float* redD = (LAS float*)(L + RED_D);
  LAS float* xst = (LAS float*)(L + XST);
  const v4u S4 = {SENT, SENT, SENT, SENT};
  const GAS u32* epp = (const GAS u32*)(p.ws + OFF_EPOCH);

  auto uk_dma = [&](int i0, int i1) {
#pragma unroll
    for (int i = i0; i < i1; ++i) {
      const int row = 4 * i + (lane >> 4);
      const int ch = (lane & 15) ^ fsw(row);
      dma16_nt(p.wuk + ((size_t)(128 * j + row) * 32 + h) * 128 + ch * 8, UKS + 4 * i * 256);
    }
    SCHED();
  };

  U16 uq;
  u32 ep;
  v4u fq[8];
  if (w < 5) {
    if (isA) {
      U16 ua;
      const v4u cfr = gld16(p.c + (n & 3) * 640 + 320 * khA + 64 * w + 32 * ((n >> 2) & 1) + 8 * g);
      load_u16w<true>(ua, p.wmod + (size_t)(320 * khA + 64 * w) * 3840 + 640 * matA + 64 * slA, 3840, lane);
      SCHED();
      const v4u af = silu_frag(cfr);
      v4u fa[8];
      tr_u16(ua, fa);
      STAMP(19);
      f32x4 acc[8];
#pragma unroll
      for (int q = 0; q < 8; ++q) acc[q] = mfma16(af, fa[q], f32x4{0, 0, 0, 0});
      if (g < 2 && (n >> 3) == g) {
#pragma unroll
        for (int b = 0; b < 4; ++b) {
          *(LAS f32x4*)(redA + (w * 2 + g) * RS + b * 64 + 8 * (n & 7)) = f32x4{acc[0][b], acc[1][b], acc[2][b], acc[3][b]};
          *(LAS f32x4*)(redA + (w * 2 + g) * RS + b * 64 + 8 * (n & 7) + 4) = f32x4{acc[4][b], acc[5][b], acc[6][b], acc[7][b]};
        }
      }
      SCHED();
    }
    load_u16w<true>(uq, p.wq + (size_t)(320 * kh + 64 * w) * 4096 + h * 128 + 64 * dh, 4096, lane);
    ep = __hip_atomic_load(epp, __ATOMIC_RELAXED, __HIP_MEMORY_SCOPE_AGENT);
    SCHED();
    STAMP(1);
    if (isA) BAR();
  } else if (w == 7) {
    ep = __hip_atomic_load(epp, __ATOMIC_RELAXED, __HIP_MEMORY_SCOPE_AGENT);
    asm volatile("" ::: "memory");
    st16(rs, OFF_QABS + (lane >> 4) * 32768 + h * 1024 + j * 256 + (lane & 15) * 16, S4);
    if (lane < 32) st16(rs, OFF_CTX + chf * 32768 + (2 * bp + (lane >> 4)) * 8192 + h * 256 + (lane & 15) * 16, S4);
    else if (lane < 40) st16(rs, OFF_STATS + chi * 128 + (lane - 32) * 16, S4);
    if (lane < 32) { if (chi < 80) st16(rs, OFF_ATTN + chi * 512 + lane * 16, S4); }
    else { if (chi < 40) st16(rs, OFF_MODR + chi * 512 + (lane - 32) * 16, S4); }
    if (isE) {
#pragma unroll
      for (int i = 0; i < 5; ++i) st16(rs, OFF_OUTP + ((8 * i + (lane >> 3)) * 108 + e) * 128 + (lane & 7) * 16, S4);
    }
    STAMP(1);
    if (isA) BAR();
    DRAIN();
    if (lane == 0) st4(rs, OFF_FLAG1 + chi * 4, ep + 1u);
    STAMP(2);
    { const int l20 = lane < 20 ? lane : 0; trigt(rs, OFF_H + ((l20 / 5) * 10 + 5 * kh + (l20 % 5)) * 1024 + 1020, ep + 1u); }
  } else if (w == 6) {
    ep = __hip_atomic_load(epp, __ATOMIC_RELAXED, __HIP_MEMORY_SCOPE_AGENT);
    if (isA) {
      u32 d[4][5];
#pragma unroll
      for (int b = 0; b < 4; ++b)
#pragma unroll
        for (int i = 0; i < 5; ++i) d[b][i] = *(const GAS u32*)(p.x + b * 640 + 2 * (lane + 64 * i));
      SCHED();
#pragma unroll
      for (int b = 0; b < 4; ++b) {
        float s = 0.f;
#pragma unroll
        for (int i = 0; i < 5; ++i) s += bflo(d[b][i]) + bfhi(d[b][i]);
        const float m = wave_sum(s) * (1.f / 640.f);
        float q2 = 0.f;
#pragma unroll
        for (int i = 0; i < 5; ++i) { const float d0 = bflo(d[b][i]) - m, d1 = bfhi(d[b][i]) - m; q2 += d0 * d0 + d1 * d1; }
        const float var = wave_sum(q2) * (1.f / 640.f);
        if (lane == 0) { xst[2 * b] = m; xst[2 * b + 1] = rsqrtf(var + 1e-5f); }
      }
      STAMP(1);
      BAR();
    }
    uk_dma(0, 32);
  } else {
    ep = __hip_atomic_load(epp, __ATOMIC_RELAXED, __HIP_MEMORY_SCOPE_AGENT);
    if (isA) {
      const v2u xa2 = gld8(p.x + (lane >> 4) * 640 + 64 * slA + 4 * (lane & 15));
      BAR();
      f32x4 v = reduce256w<10, RS>(redA, lane);
      if (matA) {
        const float m = xst[2 * (lane >> 4)], rstd = xst[2 * (lane >> 4) + 1], one = khA == 0 ? 1.f : 0.f;
        v[0] = (bflo(xa2.x) - m) * rstd * (v[0] + one); v[1] = (bfhi(xa2.x) - m) * rstd * (v[1] + one);
        v[2] = (bflo(xa2.y) - m) * rstd * (v[2] + one); v[3] = (bfhi(xa2.y) - m) * rstd * (v[3] + one);
      }
      st16(rs, OFF_H + ((matA + 2 * khA) * 10 + slA) * 1024 + lane * 16, v4u{pk2(v[0], v[1]), ep + 1u, pk2(v[2], v[3]), ep + 1u});
      STAMP(2);
    }
  }
  const u32 tag = ep + 1u;
  if (w < 5) {
    tr_u16(uq, fq);
#pragma unroll
    for (int q = 0; q < 8; ++q) PIN4(fq[q]);
  }
  STAMP(3);
  BAR();
  STAMP(20);
  if (w < 5) {
    const int o = OFF_H + ((n >> 3) * 10 + 5 * kh + w) * 1024 + (n & 3) * 256 + 128 * ((n >> 2) & 1) + 32 * g;
    int off[4] = {o, o + 16, o + 20480, o + 20480 + 16};
    v4u a[4];
    pollg<4>(rs, off, a, tag);
    STAMP(4);
    const v4u af0 = {a[0].x, a[0].z, a[1].x, a[1].z}, af1 = {a[2].x, a[2].z, a[3].x, a[3].z};
    f32x4 acc[8];
#pragma unroll
    for (int q = 0; q < 8; ++q) { acc[q] = mfma16(af0, fq[q], f32x4{0, 0, 0, 0}); acc[q] = mfma16(af1, fq[q], acc[q]); }
    if ((n >> 3) == (g & 1)) {
#pragma unroll
      for (int b = 0; b < 4; ++b) {
        *(LAS f32x4*)(redQ + (w * 4 + g) * RS + b * 64 + 8 * (n & 7)) = f32x4{acc[0][b], acc[1][b], acc[2][b], acc[3][b]};
        *(LAS f32x4*)(redQ + (w * 4 + g) * RS + b * 64 + 8 * (n & 7) + 4) = f32x4{acc[4][b], acc[5][b], acc[6][b], acc[7][b]};
      }
    }
  }
  BAR();
  STAMP(5);
  if (w == 5) {
    const f32x4 v = reduce256w<20, RS>(redQ, lane);
    st16(rs, OFF_Q + (h * 4 + j) * 1024 + lane * 16, v4u{pk2(v[0], v[1]), tag, pk2(v[2], v[3]), tag});
  }
  STAMP(6);
  {
    if (w < 4) {
      int off[1] = {OFF_Q + h * 4096 + tid * 16}; v4u a[1];
      sleep_n(QDELAY);
      pollg<1>(rs, off, a, tag);
      *(LAS v2u*)(L + QST + tid * 8) = v2u{a[0].x, a[0].z};
    } else if (w == 6) DRAIN();
    BAR();
    STAMP(7);
    f32x4 acc = {0, 0, 0, 0};
    const int row = 16 * w + n;
    v4u af[4], bfr[4];
#pragma unroll
    for (int t = 0; t < 4; ++t) {
      af[t] = *(LAS v4u*)(L + QST + ((((n >> 2) & 1) << 1) + (t >> 1)) * 512 + (n & 3) * 128 + (32 * (t & 1) + 8 * g) * 2);
      bfr[t] = *(LAS v4u*)(L + UKS + row * 256 + (((4 * t + g) ^ fsw(row)) << 4));
    }
#pragma unroll
    for (int t = 0; t < 4; ++t) acc = mfma16(af[t], bfr[t], acc);
    float v;
    {
      const float s0 = x16_sum(acc[0]), s1 = x16_sum(acc[1]), s2 = x16_sum(acc[2]), s3 = x16_sum(acc[3]);
      v = (g == 0 ? s0 : g == 1 ? s1 : g == 2 ? s2 : s3) * (0.08838834764831845f * 1.4426950408889634f);
    }
    LAS u16* stg = (LAS u16*)(L + STG);
    stg[g * 128 + 16 * w + n] = (u16)(pk2(v, 0.f) & 0xFFFFu);
  }
  BAR();
  if (w == 0) {
    const v4u v = *(LAS v4u*)(L + STG + lane * 16);
    st16(rs, OFF_QABS + (lane >> 4) * 32768 + h * 1024 + j * 256 + (lane & 15) * 16, san4(v));
  }
  STAMP(8);
  U16 uv; U8 ug;
  {
    const u16* pv = p.wuv + ((size_t)(256 * chf + 32 * w + 8 * g) * 32 + h) * 128 + 8 * n;
#pragma unroll
    for (int i = 0; i < 8; ++i) uv.w[i] = gld16nt(pv + (size_t)i * 4096);
    if (w < 5 && isE) load_u8<true>(ug, p.wg + (size_t)(128 * w) * 1728 + 16 * e, 1728, lane);
    SCHED();
  }
  STAMP(9);
  if (w == 7) poll_flags128(rs, OFF_FLAG1, lane, tag);
  v4u fv[8];
  tr_u16(uv, fv);
#pragma unroll
  for (int q = 0; q < 8; ++q) PIN4(fv[q]);
  v4u fg[4];
  if (w < 5 && isE) {
    tr_u8(ug, fg);
#pragma unroll
    for (int q = 0; q < 4; ++q) PIN4(fg[q]);
  }
  BAR();
  STAMP(10);
  {
    int off[4]; v4u a[4];
#pragma unroll
    for (int mt = 0; mt < 4; ++mt) off[mt] = OFF_O + (((2 * bp + (mt >> 1)) * 32 + h) * 2 + chf) * 16384 + (16 * (mt & 1) + n) * 512 + 64 * w + 16 * g;
    const int so = OFF_STATS + ((2 * bp + (lane >> 5)) * 32 + (lane & 31)) * 128 + h * 4;
    if (w == 0) trig4(rs, so);
    BAR();
    STAMP(11);
    const u32 lse_u = ld4(rs, so);
#pragma unroll
    for (int mt = 0; mt < 4; ++mt) a[mt] = ld16(rs, off[mt]);
    SCHED();
    const float lse = u2f(lse_u);
    const float mx = x16_max(row_max(lse));
    const float ex = exp2f(lse - mx);
    const float al = ex / x16_sum(row_sum(ex));
    float wt[2][8];
#pragma unroll
    for (int bb = 0; bb < 2; ++bb)
#pragma unroll
      for (int mt = 0; mt < 2; ++mt)
#pragma unroll
        for (int i = 0; i < 4; ++i) wt[bb][mt * 4 + i] = __shfl(al, 32 * bb + 16 * mt + 4 * g + i);
#pragma unroll
    for (int bb = 0; bb < 2; ++bb) {
      f32x4 acc[2][8];
#pragma unroll
      for (int mt = 0; mt < 2; ++mt)
#pragma unroll
        for (int q = 0; q < 8; ++q) acc[mt][q] = mfma16(a[2 * bb + mt], fv[q], f32x4{0, 0, 0, 0});
      if (bb == 0) STAMP(16);
      const f32x4 w0 = {wt[bb][0], wt[bb][1], wt[bb][2], wt[bb][3]}, w1 = {wt[bb][4], wt[bb][5], wt[bb][6], wt[bb][7]};
      float res[8];
#pragma unroll
      for (int q = 0; q < 8; ++q) { const f32x4 t = acc[0][q] * w0 + acc[1][q] * w1; res[q] = (t[0] + t[1]) + (t[2] + t[3]); }
      *(LAS f32x4*)(redD + (w * 4 + g) * RS + bb * 128 + 8 * n) = f32x4{res[0], res[1], res[2], res[3]};
      *(LAS f32x4*)(redD + (w * 4 + g) * RS + bb * 128 + 8 * n + 4) = f32x4{res[4], res[5], res[6], res[7]};
    }
  }
  STAMP(17);
  BAR();
  if (w == 5) {
    const f32x4 v = reduce256w<32, RS>(redD, lane);
    const v4u o = pack8_pairs(v);
    if (!(lane & 1)) st16(rs, OFF_CTX + chf * 32768 + (2 * bp + (lane >> 5)) * 8192 + h * 256 + (lane & 31) * 8, san4(o));
  }
  STAMP(12);
  if (isE) {
    U8 uu; U16 ud;
    if (w < 5) { load_u8<true>(uu, p.wu + (size_t)(128 * w) * 1728 + 16 * e, 1728, lane); SCHED(); }
    else {
      const int col = 256 * (w - 5) + 128 * (g >> 1) + 8 * n;
      const u16* pd = p.wd + (size_t)(16 * e + 8 * (g & 1)) * 640 + (col < 640 ? col : 0);
#pragma unroll
      for (int i = 0; i < 8; ++i) ud.w[i] = gld16nt(pd + (size_t)i * 640);
      SCHED();
    }
    const int eb = w >> 1, ec0 = 320 * (w & 1) + 8 * (lane < 40 ? lane : 39);
    v4u mga, msb, mcb, xe;
    {
      int off[3]; v4u a[3];
#pragma unroll
      for (int k = 0; k < 3; ++k) off[k] = OFF_MODR + (k * 10 + (ec0 >> 6)) * 512 + eb * 128 + (ec0 & 63) * 2;
      xe = gld16(p.x + eb * 640 + ec0);
      poll16<3>(rs, off, a);
      mga = a[0]; msb = a[1]; mcb = a[2];
    }
    v4u fu[4], fd[8];
    if (w < 5) {
      tr_u8(uu, fu);
#pragma unroll
      for (int q = 0; q < 4; ++q) PIN4(fu[q]);
    } else {
      tr_u16(ud, fd);
#pragma unroll
      for (int q = 0; q < 8; ++q) PIN4(fd[q]);
    }
    STAMP(13);
    int off[8]; v4u a[8];
#pragma unroll
    for (int k = 0; k < 8; ++k) off[k] = OFF_ATTN + (k * 10 + (ec0 >> 6)) * 512 + eb * 128 + (ec0 & 63) * 2;
    if (w < 2) trig4(rs, OFF_ATTN + (tid < 80 ? tid : 79) * 512 + 508);
    BAR();
    poll16<8>(rs, off, a);
    STAMP(14);
    float x1[8], s = 0.f;
#pragma unroll
    for (int t = 0; t < 4; ++t) {
      float lo = 0.f, hi = 0.f;
#pragma unroll
      for (int k = 0; k < 8; ++k) { lo += bflo(a[k][t]); hi += bfhi(a[k][t]); }
      x1[2 * t] = bflo(xe[t]) + bflo(mga[t]) * lo;
      x1[2 * t + 1] = bfhi(xe[t]) + bfhi(mga[t]) * hi;
      s += x1[2 * t] + x1[2 * t + 1];
    }
    if (lane >= 40) s = 0.f;
    const float mw = wave_sum(s) * (1.f / 320.f);
    float q2 = 0.f;
#pragma unroll
    for (int i = 0; i < 8; ++i) { const float d = x1[i] - mw; q2 += d * d; }
    if (lane >= 40) q2 = 0.f;
    q2 = wave_sum(q2);
    LAS float* xs = (LAS float*)(L + XE);
    if (lane == 0) { xs[2 * w] = mw; xs[2 * w + 1] = q2; }
    BAR();
    const float mo = xs[2 * (w ^ 1)], qo = xs[2 * (w ^ 1) + 1];
    const float m = 0.5f * (mw + mo);
    const float rstd = rsqrtf((q2 + qo + 160.f * (mw - mo) * (mw - mo)) * (1.f / 640.f) + 1e-5f);
    v4u hv;
#pragma unroll
    for (int t = 0; t < 4; ++t) {
      const float lo = (x1[2 * t] - m) * rstd * (1.f + bflo(mcb[t])) + bflo(msb[t]);
      const float hi = (x1[2 * t + 1] - m) * rstd * (1.f + bfhi(mcb[t])) + bfhi(msb[t]);
      hv[t] = pk2(lo, hi);
    }
    if (lane < 40) *(LAS v4u*)(L + HS + eb * 1280 + ec0 * 2) = hv;
    BAR();
    if (w < 5) {
      const v4u af = *(LAS v4u*)(L + HS + (n & 3) * 1280 + (128 * w + 32 * (n >> 2) + 8 * g) * 2);
      f32x4 ag[4], au[4];
#pragma unroll
      for (int q = 0; q < 4; ++q) { ag[q] = mfma16(af, fg[q], f32x4{0, 0, 0, 0}); au[q] = mfma16(af, fu[q], f32x4{0, 0, 0, 0}); }
      if ((n >> 2) == g) {
#pragma unroll
        for (int b = 0; b < 4; ++b) {
          *(LAS f32x4*)(redE + (w * 4 + g) * 128 + b * 16 + 4 * (n & 3)) = f32x4{ag[0][b], ag[1][b], ag[2][b], ag[3][b]};
          *(LAS f32x4*)(redE + (w * 4 + g) * 128 + 64 + b * 16 + 4 * (n & 3)) = f32x4{au[0][b], au[1][b], au[2][b], au[3][b]};
        }
      }
    }
    BAR();
    STAMP(15);
    if (w >= 5) {
      f32x4 gg = {0, 0, 0, 0}, uu2 = {0, 0, 0, 0};
#pragma unroll
      for (int k = 0; k < 5; ++k) { gg += *(LAS f32x4*)(redE + (4 * k + g) * 128 + 4 * n); uu2 += *(LAS f32x4*)(redE + (4 * k + g) * 128 + 64 + 4 * n); }
      float hd[4];
#pragma unroll
      for (int c = 0; c < 4; ++c) {
        const float gv = x32_sum(x16_sum(gg[c])), uv2 = x32_sum(x16_sum(uu2[c]));
        const float y = 0.7978845608028654f * (gv + 0.044715f * gv * gv * gv);
        const float th = 1.f - 2.f / (__expf(2.f * y) + 1.f);
        hd[c] = 0.5f * gv * (1.f + th) * uv2;
      }
      LAS unsigned char* hb = L + HID + (w - 5) * 128;
      if (g == 0) *(LAS v2u*)(hb + n * 8) = v2u{pk2(hd[0], hd[1]), pk2(hd[2], hd[3])};
      LGKM0();
      v4u af = *(LAS v4u*)(hb + (n & 3) * 32 + (g & 1) * 16);
      if ((g >> 1) != ((n >> 2) & 1)) af = v4u{0, 0, 0, 0};
      f32x4 acc[8];
#pragma unroll
      for (int q = 0; q < 8; ++q) acc[q] = mfma16(af, fd[q], f32x4{0, 0, 0, 0});
      LAS unsigned char* ob = L + OST + (w - 5) * 2048;
      if (g < 2) {
#pragma unroll
        for (int b = 0; b < 4; ++b) {
          v4u o;
#pragma unroll
          for (int t = 0; t < 4; ++t) o[t] = pk2(acc[2 * t][b], acc[2 * t + 1][b]);
          *(LAS v4u*)(ob + (8 * g + (n >> 1)) * 128 + b * 32 + (n & 1) * 16) = san4(o);
        }
      }
      LGKM0();
#pragma unroll
      for (int i = 0; i < 2; ++i) {
        const int ln = 8 * i + (lane >> 3), grp = 16 * (w - 5) + ln;
        const v4u o = *(LAS v4u*)(ob + ln * 128 + (lane & 7) * 16);
        if (grp < 40) st16(rs, OFF_OUTP + (grp * 108 + e) * 128 + (lane & 7) * 16, o);
      }
    }
    STAMP(18);
  }
  STAMP_FLUSH();
}

DEV void att_role(const Params& p, LAS unsigned char* L, rsrc_t rs, int x, int r, int tid, int lane, int w) {
  STAMP_DECL
  STAMP(0);
  const int b = x >> 1, s = ((x & 1) << 4) | r;
  const bool isD3 = r < 10;
  const int id3 = 10 * x + r, ks3 = id3 / 10, slab3 = id3 % 10;
  const bool isM = r >= 10 && r < 15;
  const int slabM = 5 * x + (r - 10);
  const int g = lane >> 4, n = lane & 15, hh = lane >> 5, m32 = lane & 31;
  const int G = 8 * w + (lane & 7);
  constexpr int QS = 131072;
  __builtin_amdgcn_s_sleep(ATT_DELAY);
  {
    const u16* cb = p.ckv + ((size_t)b * 4096 + s * 128 + 16 * w) * 512;
#pragma unroll
    for (int i = 0; i < 16; ++i) {
      const int row = 16 * w + i;
      const int ch = (lane & ~15) | ((lane & 15) ^ fsw(row));
      dma16_nt(cb + (size_t)i * 512 + ch * 8, row * 1024);
    }
  }
  const u32 tag = __hip_atomic_load((GAS u32*)(p.ws + OFF_EPOCH), __ATOMIC_RELAXED, __HIP_MEMORY_SCOPE_AGENT) + 1u;
  STAMP(1);
  if (w == 7) poll_flags128(rs, OFF_FLAG1, lane, tag);
  DRAIN();
  BAR();
  STAMP(2);
  v4u ka[32];
  if (w < 4) {
    const int row = 32 * w + m32;
    const int a0 = row * 1024 + ((hh ^ fsw(row)) << 4);
#pragma unroll
    for (int t = 0; t < 32; ++t) ka[t] = *(LAS v4u*)(L + ((a0 ^ ((t & 7) << 5)) + (t >> 3) * 256));
  }
  v4u pa[2][8];
  const int tq = (lane & 15) >> 2, tp = lane & 3, tcb = g & 1;
  auto read_pa = [&]() {
#pragma unroll
    for (int mt = 0; mt < 2; ++mt)
#pragma unroll
      for (int ks = 0; ks < 8; ++ks)
#pragma unroll
        for (int t2 = 0; t2 < 2; ++t2) {
          const int row = 16 * ks + 8 * t2 + 4 * hh + tq;
          const int ch = 8 * w + 4 * mt + 2 * tcb + (tp >> 1);
          const int f = (tq << 2) | ((2 * t2 + hh) & 3);
          const int addr = row * 1024 + ((ch ^ f) << 4) + 8 * (tp & 1);
          const s16x4 v = __builtin_amdgcn_ds_read_tr16_b64_v4i16((LAS s16x4*)(L + addr));
          const v2u u = __builtin_bit_cast(v2u, v);
          pa[mt][ks][2 * t2] = u.x; pa[mt][ks][2 * t2 + 1] = u.y;
        }
  };
  {
    int off[4]; v4u a[4];
#pragma unroll
    for (int i = 0; i < 4; ++i) off[i] = OFF_QABS + b * 32768 + (tid + 512 * i) * 16;
    if (w >= 4) read_pa();
    STAMP(3);
    if (w >= 6) trig4(rs, OFF_QABS + b * 32768 + (tid - 384) * 256 + 252);
    BAR();
    STAMP(15);
    poll16<4>(rs, off, a);
    STAMP(4);
#pragma unroll
    for (int i = 0; i < 4; ++i) {
      const int head = w + 8 * i, ch = lane;
      *(LAS v4u*)(L + QS + ch * 512 + ((head ^ (ch & 7)) << 4)) = a[i];
    }
  }
  BAR();
  STAMP(5);
  auto scr = [&](int off) { return (off >> 9) * 1024 + 512 + (off & 511); };
  constexpr int SC_P = 0, SC_MX = 8192, SC_LS = 8704;
  f32x16 sacc;
  if (w < 4) {
#pragma unroll
    for (int i = 0; i < 16; ++i) sacc[i] = 0.f;
    auto rdq = [&](int t) { const int ch = 2 * t + hh; return *(LAS v4u*)(L + QS + ch * 512 + ((m32 ^ (ch & 7)) << 4)); };
    v4u qb[8];
#pragma unroll
    for (int t = 0; t < 8; ++t) qb[t] = rdq(t);
    SCHED();
#pragma unroll
    for (int t = 0; t < 32; ++t) {
      const v4u cur = qb[t & 7];
      sacc = mfma32(ka[t], cur, sacc);
      if (t + 8 < 32) qb[t & 7] = rdq(t + 8);
      SCHED();
    }
    float mx = sacc[0];
#pragma unroll
    for (int i = 1; i < 16; ++i) mx = fmaxf(mx, sacc[i]);
    mx = x32_max(mx);
    if (hh == 0) *(LAS float*)(L + scr(SC_MX + (w * 32 + m32) * 4)) = mx;
    STAMP(6);
  }
  BAR();
  STAMP(7);
  float M;
  {
    float m0 = *(LAS float*)(L + scr(SC_MX + (0 * 32 + m32) * 4)), m1 = *(LAS float*)(L + scr(SC_MX + (1 * 32 + m32) * 4));
    float m2 = *(LAS float*)(L + scr(SC_MX + (2 * 32 + m32) * 4)), m3 = *(LAS float*)(L + scr(SC_MX + (3 * 32 + m32) * 4));
    M = fmaxf(fmaxf(m0, m1), fmaxf(m2, m3));
  }
  if (w < 4) {
    float pr[16], ls = 0.f;
#pragma unroll
    for (int i = 0; i < 16; ++i) { pr[i] = __builtin_amdgcn_exp2f(sacc[i] - M); ls += pr[i]; }
    ls = x32_sum(ls);
    if (hh == 0) *(LAS float*)(L + scr(SC_LS + (w * 32 + m32) * 4)) = ls;
#pragma unroll
    for (int sp = 0; sp < 2; ++sp) {
      v4u pf;
#pragma unroll
      for (int t = 0; t < 4; ++t) pf[t] = pk2(pr[8 * sp + 2 * t], pr[8 * sp + 2 * t + 1]);
      *(LAS v4u*)(L + scr(SC_P + ((2 * w + sp) * 64 + lane) * 16)) = pf;
    }
    read_pa();
  }
  BAR();
  STAMP(8);
  float lsum;
  {
    float l0 = *(LAS float*)(L + scr(SC_LS + (0 * 32 + m32) * 4)), l1 = *(LAS float*)(L + scr(SC_LS + (1 * 32 + m32) * 4));
    float l2 = *(LAS float*)(L + scr(SC_LS + (2 * 32 + m32) * 4)), l3 = *(LAS float*)(L + scr(SC_LS + (3 * 32 + m32) * 4));
    lsum = (l0 + l1) + (l2 + l3);
  }
  f32x16 oacc[2];
#pragma unroll
  for (int mt = 0; mt < 2; ++mt)
#pragma unroll
    for (int i = 0; i < 16; ++i) oacc[mt][i] = 0.f;
  {
    v4u pf[8];
#pragma unroll
    for (int ks = 0; ks < 8; ++ks) pf[ks] = *(LAS v4u*)(L + scr(SC_P + (ks * 64 + lane) * 16));
    SCHED();
#pragma unroll
    for (int ks = 0; ks < 8; ++ks) {
      oacc[0] = mfma32(pa[0][ks], pf[ks], oacc[0]);
      oacc[1] = mfma32(pa[1][ks], pf[ks], oacc[1]);
    }
  }
  STAMP(9);
  {
    const float inv = 1.f / lsum;
    LAS unsigned char* st = L + QS + w * 4096;
#pragma unroll
    for (int mt = 0; mt < 2; ++mt)
#pragma unroll
      for (int qd = 0; qd < 4; ++qd) {
        v2u o;
        o.x = pk2(oacc[mt][4 * qd] * inv, oacc[mt][4 * qd + 1] * inv);
        o.y = pk2(oacc[mt][4 * qd + 2] * inv, oacc[mt][4 * qd + 3] * inv);
        *(LAS v2u*)(st + m32 * 128 + (((4 * mt + qd) ^ (m32 & 7)) << 4) + 8 * hh) = o;
      }
    LGKM0();
#pragma unroll
    for (int i = 0; i < 4; ++i) {
      const int head = 8 * i + (lane >> 3), ch = lane & 7;
      const v4u v = *(LAS v4u*)(st + head * 128 + ((ch ^ (head & 7)) << 4));
      st16(rs, OFF_O + ((b * 32 + head) * 2 + (w >> 2)) * 16384 + s * 512 + (w & 3) * 128 + ch * 16, v);
    }
    STAMP(16);
    DRAIN();
    BAR();
    if (w == 0) {
      const float lse = M + __log2f(lsum);
      LAS float* sf = (LAS float*)(L + scr(SC_MX + 1024));
      if (hh == 0) sf[m32] = lse;
      LGKM0();
      if (lane < 8) { const v4u v = *(LAS v4u*)(sf + 4 * lane); st16(rs, OFF_STATS + (b * 32 + s) * 128 + lane * 16, san4(v)); }
    }
  }
  STAMP(10);
  constexpr int RED_3 = 32768, RED_F = 98304;
  if (isD3) {
    U16 uo;
    load_u16w<true>(uo, p.wo + (size_t)(512 * ks3 + 64 * w) * 640 + 64 * slab3, 640, lane);
    SCHED();
    v4u fo[8];
    tr_u16(uo, fo);
#pragma unroll
    for (int q = 0; q < 8; ++q) PIN4(fo[q]);
    STAMP(11);
    const int k0 = 512 * ks3 + 64 * w + 32 * ((n >> 2) & 1) + 8 * g;
    int off[1] = {OFF_CTX + (n >> 3) * 32768 + (n & 3) * 8192 + k0 * 2};
    v4u a[1];
    if (w == 0) trig4(rs, OFF_CTX + ((lane >> 2) & 1) * 32768 + ((lane >> 3) & 3) * 8192 + (4 * ks3 + (lane & 3)) * 256 + 252);
    if (w == 0 && id3 == 0 && lane == 0) st4(rs, OFF_EPOCH, tag);
    BAR();
    poll16<1>(rs, off, a);
    STAMP(12);
    f32x4 acc[8];
#pragma unroll
    for (int q = 0; q < 8; ++q) acc[q] = mfma16(a[0], fo[q], f32x4{0, 0, 0, 0});
    LAS float* red = (LAS float*)(L + RED_3);
    if ((n >> 3) == (g & 1)) {
#pragma unroll
      for (int bb = 0; bb < 4; ++bb) {
        *(LAS f32x4*)(red + (w * 4 + g) * RS + bb * 64 + 8 * (n & 7)) = f32x4{acc[0][bb], acc[1][bb], acc[2][bb], acc[3][bb]};
        *(LAS f32x4*)(red + (w * 4 + g) * RS + bb * 64 + 8 * (n & 7) + 4) = f32x4{acc[4][bb], acc[5][bb], acc[6][bb], acc[7][bb]};
      }
    }
    BAR();
    if (w == 0) {
      const f32x4 v = reduce256w<32, RS>(red, lane);
      const v4u o = pack8_pairs(v);
      if (!(lane & 1)) st16(rs, OFF_ATTN + id3 * 512 + lane * 8, san4(o));
    }
    STAMP(13);
  }
  if (isM) {
    U16 um[2]; v4u cf[2];
    LAS float* red = (LAS float*)(L + RED_3);
    if (w < 5) {
#pragma unroll
      for (int u = 0; u < 2; ++u) {
        cf[u] = gld16(p.c + (n & 3) * 640 + 128 * w + 64 * u + 32 * ((n >> 2) & 1) + 8 * g);
        load_u16w<true>(um[u], p.wmod + (size_t)(128 * w + 64 * u) * 3840 + 1280 + 64 * slabM, 3840, lane);
      }
      SCHED();
      f32x4 acc[8];
#pragma unroll
      for (int q = 0; q < 8; ++q) acc[q] = f32x4{0, 0, 0, 0};
#pragma unroll
      for (int u = 0; u < 2; ++u) {
        v4u fm[8];
        tr_u16(um[u], fm);
        const v4u af = silu_frag(cf[u]);
#pragma unroll
        for (int q = 0; q < 8; ++q) acc[q] = mfma16(af, fm[q], acc[q]);
      }
      if (g < 2 && (n >> 3) == g) {
#pragma unroll
        for (int bb = 0; bb < 4; ++bb) {
          *(LAS f32x4*)(red + (w * 2 + g) * RS + bb * 64 + 8 * (n & 7)) = f32x4{acc[0][bb], acc[1][bb], acc[2][bb], acc[3][bb]};
          *(LAS f32x4*)(red + (w * 2 + g) * RS + bb * 64 + 8 * (n & 7) + 4) = f32x4{acc[4][bb], acc[5][bb], acc[6][bb], acc[7][bb]};
        }
      }
    }
    BAR();
    if (w == 1) {
      const f32x4 v = reduce256w<10, RS>(red, lane);
      const v4u o = pack8_pairs(v);
      if (!(lane & 1)) st16(rs, OFF_MODR + slabM * 512 + lane * 8, san4(o));
    }
    STAMP(11);
    float x1 = 0.f, gbv = 0.f;
    if (w == 0) {
      const int bb = lane >> 4, col = 16 * slabM + (lane & 15);
      const int sub = bb * 128 + ((col & 63) * 2 & ~3);
      const u32 xv = (u32)p.x[bb * 640 + col];
      u32 v[10];
      for (int spins = 0;; ++spins) {
        asm volatile("" ::: "memory");
#pragma unroll
        for (int k = 0; k < 8; ++k) v[k] = ld4(rs, OFF_ATTN + (k * 10 + (col >> 6)) * 512 + sub);
        v[8] = ld4(rs, OFF_MODR + (0 * 10 + (col >> 6)) * 512 + sub);
        v[9] = ld4(rs, OFF_MODR + (3 * 10 + (col >> 6)) * 512 + sub);
        bool ok = true;
#pragma unroll
        for (int k = 0; k < 10; ++k) ok = ok && v[k] != SENT;
        if (__all(ok) || spins > SPIN_CAP) break;
        __builtin_amdgcn_s_sleep(4);
      }
      float f[10], at = 0.f;
#pragma unroll
      for (int k = 0; k < 10; ++k) f[k] = (col & 1) ? bfhi(v[k]) : bflo(v[k]);
#pragma unroll
      for (int k = 0; k < 8; ++k) at += f[k];
      x1 = bflo(xv) + f[8] * at;
      gbv = f[9];
    }
    BAR();
    STAMP(12);
    const int e0 = tid >> 3, e1 = (64 + e0 < 108) ? 64 + e0 : e0;
    int off[2] = {OFF_OUTP + (slabM * 108 + e0) * 128 + (tid & 7) * 16, OFF_OUTP + (slabM * 108 + e1) * 128 + (tid & 7) * 16};
    v4u a[2];
    if (w >= 6) { const int t2 = tid - 384; trig4(rs, OFF_OUTP + (slabM * 108 + (t2 < 108 ? t2 : 107)) * 128 + 124); }
    BAR();
    poll16<2>(rs, off, a);
    STAMP(13);
    float v8[8];
    const float m1 = (64 + e0 < 108) ? 1.f : 0.f;
#pragma unroll
    for (int t = 0; t < 4; ++t) { v8[2 * t] = bflo(a[0][t]) + m1 * bflo(a[1][t]); v8[2 * t + 1] = bfhi(a[0][t]) + m1 * bfhi(a[1][t]); }
#pragma unroll
    for (int t = 0; t < 8; ++t) { float v = v8[t]; v += dppf<0x128>(v); v = x32_sum(x16_sum(v)); v8[t] = v; }
    LAS float* redf = (LAS float*)(L + RED_F);
    if (lane < 8) {
      *(LAS f32x4*)(redf + w * 64 + lane * 8) = f32x4{v8[0], v8[1], v8[2], v8[3]};
      *(LAS f32x4*)(redf + w * 64 + lane * 8 + 4) = f32x4{v8[4], v8[5], v8[6], v8[7]};
    }
    BAR();
    if (w == 0) {
      float y = 0.f;
#pragma unroll
      for (int i = 0; i < 8; ++i) y += redf[i * 64 + lane];
      const float o = x1 + gbv * y;
      p.out[(lane >> 4) * 640 + 16 * slabM + (lane & 15)] = (u16)(pk2(o, 0.f) & 0xFFFFu);
    }
    STAMP(17);
  }
  STAMP_FLUSH();
}

__global__ void __launch_bounds__(512, 2) mk_kernel(Params p) {
  extern __shared__ __attribute__((aligned(16))) unsigned char smem[];
  LAS unsigned char* L = (LAS unsigned char*)smem;
  const int tid = threadIdx.x, lane = tid & 63;
  const int w = __builtin_amdgcn_readfirstlane(tid >> 6);
  const int x = blockIdx.x & 7, r = blockIdx.x >> 3;
  const rsrc_t rs = __builtin_amdgcn_make_buffer_rsrc((void*)p.ws, 0, WS_USED, 0x00020000);
  v4u cpf0 = {0, 0, 0, 0}, cpf1 = {0, 0, 0, 0};
  if (w == 5) {
    unsigned long long pc;
    asm volatile("s_getpc_b64 %0" : "=s"(pc));
    const unsigned char* base = (const unsigned char*)(pc & ~255ull);
    const int o0 = r * 1024 + lane * 16, o1 = 32768 + r * 1024 + lane * 16;
    cpf0 = *(const GAS v4u*)(base + o0);
    if (o1 + 16 <= CODE_BYTES) cpf1 = *(const GAS v4u*)(base + o1);
    asm volatile("" :: "v"(cpf0), "v"(cpf1));
  }
  if (r < 16) ch_role(p, L, rs, x, r, tid, lane, w);
  else att_role(p, L, rs, x, r - 16, tid, lane, w);
}
}

extern "C" void kernel_launch(void* const* d_in, const int* in_sizes, int n_in, void* d_out, int out_size, void* d_ws, size_t ws_size, hipStream_t stream) {
  Params p;
  p.x = (const u16*)d_in[0]; p.ckv = (const u16*)d_in[1]; p.wq = (const u16*)d_in[2]; p.wuk = (const u16*)d_in[3];
  p.wuv = (const u16*)d_in[4]; p.wo = (const u16*)d_in[5]; p.wg = (const u16*)d_in[7]; p.wu = (const u16*)d_in[8];
  p.wd = (const u16*)d_in[9]; p.c = (const u16*)d_in[10]; p.wmod = (const u16*)d_in[11];
  p.out = (u16*)d_out; p.ws = (unsigned char*)d_ws;
  (void)hipFuncSetAttribute((const void*)mk_kernel, hipFuncAttributeMaxDynamicSharedMemorySize, LDS_BYTES);
  hipLaunchKernelGGL(mk_kernel, dim3(256), dim3(512), LDS_BYTES, stream, p);
}
```

```cpp
#include <hip/hip_runtime.h>
#include <stdint.h>

namespace {
typedef unsigned short u16;
typedef unsigned u32;
typedef u32 v4u __attribute__((ext_vector_type(4)));
typedef u32 v2u __attribute__((ext_vector_type(2)));
typedef float f32x4 __attribute__((ext_vector_type(4)));
typedef float f32x16 __attribute__((ext_vector_type(16)));
typedef __bf16 bf8 __attribute__((ext_vector_type(8)));
typedef __bf16 bf2 __attribute__((ext_vector_type(2)));
typedef short s16x4 __attribute__((ext_vector_type(4)));
#define GAS __attribute__((address_space(1)))
#define LAS __attribute__((address_space(3)))
#define DEV __device__ __forceinline__

struct Params {
  const u16 *x, *ckv, *wq, *wuk, *wuv, *wo, *wg, *wu, *wd, *c, *wmod;
  u16* out;
  unsigned char* ws;
};

constexpr int OFF_EPOCH = 0;
constexpr int OFF_FLAG1 = 1024;
constexpr int OFF_FLAG2 = 2048;
constexpr int OFF_H = 4096;
constexpr int OFF_Q = 65536;
constexpr int OFF_QABS = 262144;
constexpr int OFF_STATS = 393216;
constexpr int OFF_CTX = 409600;
constexpr int OFF_ATTN = 475136;
constexpr int OFF_MODR = 516096;
constexpr int OFF_HID = 536576;
constexpr int OFF_OUTP = 557056;
constexpr int OFF_O = 2 << 20;
constexpr int WS_USED = OFF_O + (4 << 20);
constexpr int OFF_DBG = 8 << 20;

constexpr u32 SENT = 0xFFFFFFFFu;
constexpr int SPIN_CAP = 40000;
constexpr int AUX_SC1 = 16;
constexpr int LDS_BYTES = 163840;
#define TLB_WARM 0
#define CODE_PREFETCH 1
constexpr int CODE_BYTES = 32768;
#define ATT_DELAY 44
#define QDELAY 4
#define HSLP 2
#define CDELAY 4
#define CH_DELAY 16

DEV float u2f(u32 u) { return __builtin_bit_cast(float, u); }
DEV u32 f2u(float f) { return __builtin_bit_cast(u32, f); }
DEV float bflo(u32 d) { return u2f(d << 16); }
DEV float bfhi(u32 d) { return u2f(d & 0xFFFF0000u); }
DEV u32 pk2(float lo, float hi) { bf2 v = {(__bf16)lo, (__bf16)hi}; return __builtin_bit_cast(u32, v); }
DEV u32 san(u32 d) { return d < 0xFFFFFFFEu ? d : 0xFFFFFFFEu; }
DEV v4u san4(v4u v) { v.x = san(v.x); v.y = san(v.y); v.z = san(v.z); v.w = san(v.w); return v; }
DEV bool ok4(v4u v) { u32 a = v.x > v.y ? v.x : v.y, b = v.z > v.w ? v.z : v.w; return (a > b ? a : b) != SENT; }
DEV f32x4 mfma16(v4u a, v4u b, f32x4 c) { return __builtin_amdgcn_mfma_f32_16x16x32_bf16(__builtin_bit_cast(bf8, a), __builtin_bit_cast(bf8, b), c, 0, 0, 0); }
DEV f32x16 mfma32(v4u a, v4u b, f32x16 c) { return __builtin_amdgcn_mfma_f32_32x32x16_bf16(__builtin_bit_cast(bf8, a), __builtin_bit_cast(bf8, b), c, 0, 0, 0); }
template <int C> DEV float dppf(float v) { return u2f((u32)__builtin_amdgcn_update_dpp(0, (int)f2u(v), C, 0xf, 0xf, false)); }
DEV float row_sum(float v) { v += dppf<0xB1>(v); v += dppf<0x4E>(v); v += dppf<0x141>(v); v += dppf<0x140>(v); return v; }
DEV float row_max(float v) { v = fmaxf(v, dppf<0xB1>(v)); v = fmaxf(v, dppf<0x4E>(v)); v = fmaxf(v, dppf<0x141>(v)); v = fmaxf(v, dppf<0x140>(v)); return v; }
DEV float x16_sum(float v) { auto r = __builtin_amdgcn_permlane16_swap(f2u(v), f2u(v), false, false); return u2f(r[0]) + u2f(r[1]); }
DEV float x32_sum(float v) { auto r = __builtin_amdgcn_permlane32_swap(f2u(v), f2u(v), false, false); return u2f(r[0]) + u2f(r[1]); }
DEV float x16_max(float v) { auto r = __builtin_amdgcn_permlane16_swap(f2u(v), f2u(v), false, false); return fmaxf(u2f(r[0]), u2f(r[1])); }
DEV float x32_max(float v) { auto r = __builtin_amdgcn_permlane32_swap(f2u(v), f2u(v), false, false); return fmaxf(u2f(r[0]), u2f(r[1])); }
DEV float wave_sum(float v) { return x32_sum(x16_sum(row_sum(v))); }
DEV float silu(float v) { return v / (1.f + __expf(-v)); }

#define BAR() do { asm volatile("s_waitcnt lgkmcnt(0)" ::: "memory"); __builtin_amdgcn_s_barrier(); asm volatile("" ::: "memory"); } while (0)
#define DRAIN() asm volatile("s_waitcnt vmcnt(0)" ::: "memory")
#define LGKM0() asm volatile("s_waitcnt lgkmcnt(0)" ::: "memory")
#define SCHED() __builtin_amdgcn_sched_barrier(0)
#define PIN4(v) asm volatile("" : "+v"(v))

typedef __amdgpu_buffer_rsrc_t rsrc_t;
DEV v4u ld16(rsrc_t rs, int off) { return __builtin_amdgcn_raw_buffer_load_b128(rs, off, 0, AUX_SC1); }
DEV u32 ld4(rsrc_t rs, int off) { return __builtin_amdgcn_raw_buffer_load_b32(rs, off, 0, AUX_SC1); }
DEV void st16(rsrc_t rs, int off, v4u v) { __builtin_amdgcn_raw_buffer_store_b128(v, rs, off, 0, AUX_SC1); }
DEV void st4(rsrc_t rs, int off, u32 v) { __builtin_amdgcn_raw_buffer_store_b32(v, rs, off, 0, AUX_SC1); }
DEV v4u gld16(const u16* p) { return *(const GAS v4u*)p; }
DEV v2u gld8(const u16* p) { return *(const GAS v2u*)p; }
DEV v4u gld16nt(const u16* p) { return __builtin_nontemporal_load((const GAS v4u*)p); }
DEV v2u gld8nt(const u16* p) { return __builtin_nontemporal_load((const GAS v2u*)p); }

DEV void dma16_nt(const void* src, int dst) {
  u32 keep;
  asm volatile("s_mov_b32 %0, m0\n\ts_mov_b32 m0, %2\n\ts_nop 0\n\tglobal_load_lds_dwordx4 %1, off nt\n\ts_mov_b32 m0, %0" : "=&s"(keep) : "v"(src), "s"(dst) : "memory");
}
template <int NP, int RS> DEV f32x4 reduce256(LAS float* red, int w, int lane) {
  const int G = 8 * w + (lane & 7), pg = lane >> 3;
  constexpr int NK = (NP + 7) / 8;
  f32x4 v[NK];
#pragma unroll
  for (int k = 0; k < NK; ++k) { const bool ok = 8 * k + 8 <= NP || pg + 8 * k < NP; v[k] = *(LAS f32x4*)(red + (ok ? pg + 8 * k : pg) * RS + 4 * G); }
  SCHED();
  f32x4 s = v[0];
#pragma unroll
  for (int k = 1; k < NK; ++k) { const bool ok = 8 * k + 8 <= NP || pg + 8 * k < NP; s += ok ? v[k] : f32x4{0, 0, 0, 0}; }
#pragma unroll
  for (int c = 0; c < 4; ++c) { float t = s[c]; t += dppf<0x128>(t); t = x32_sum(x16_sum(t)); s[c] = t; }
  return s;
}
template <int NP, int RS> DEV f32x4 reduce256w(LAS float* red, int lane) {
  f32x4 v[NP];
#pragma unroll
  for (int k = 0; k < NP; ++k) v[k] = *(LAS f32x4*)(red + k * RS + 4 * lane);
  SCHED();
#pragma unroll
  for (int st = 1; st < NP; st *= 2)
#pragma unroll
    for (int k = 0; k + st < NP; k += 2 * st) v[k] += v[k + st];
  return v[0];
}
template <int NP, int RS> DEV f32x4 reduce256q(LAS float* red, int w, int lane) {
  const int G = 16 * w + (lane & 15), pg = lane >> 4;
  constexpr int NK = (NP + 3) / 4;
  f32x4 v[NK];
#pragma unroll
  for (int k = 0; k < NK; ++k) { const bool ok = 4 * k + 4 <= NP || pg + 4 * k < NP; v[k] = *(LAS f32x4*)(red + (ok ? pg + 4 * k : pg) * RS + 4 * G); }
  SCHED();
  f32x4 s = v[0];
#pragma unroll
  for (int k = 1; k < NK; ++k) { const bool ok = 4 * k + 4 <= NP || pg + 4 * k < NP; s += ok ? v[k] : f32x4{0, 0, 0, 0}; }
#pragma unroll
  for (int c = 0; c < 4; ++c) s[c] = x32_sum(x16_sum(s[c]));
  return s;
}
DEV v4u pack8_pairs(f32x4 s) {
  const u32 a = pk2(s[0], s[1]), b = pk2(s[2], s[3]);
  const u32 na = (u32)__builtin_amdgcn_update_dpp(0, (int)a, 0xB1, 0xf, 0xf, false), nb = (u32)__builtin_amdgcn_update_dpp(0, (int)b, 0xB1, 0xf, 0xf, false);
  return v4u{a, b, na, nb};
}
DEV void sleep_n(int n) { for (int i = 0; i < n; ++i) __builtin_amdgcn_s_sleep(1); }

template <int N> DEV void poll16(rsrc_t rs, const int (&off)[N], v4u (&v)[N]) {
  for (int spins = 0;; ++spins) {
    asm volatile("" ::: "memory");
#pragma unroll
    for (int i = 0; i < N; ++i) v[i] = ld16(rs, off[i]);
    bool ok = true;
#pragma unroll
    for (int i = 0; i < N; ++i) ok = ok && ok4(v[i]);
    if (__all(ok) || spins > SPIN_CAP) break;
  }
}
template <int N> DEV void poll16s(rsrc_t rs, const int (&off)[N], v4u (&v)[N], int slp) {
  for (int spins = 0;; ++spins) {
    asm volatile("" ::: "memory");
#pragma unroll
    for (int i = 0; i < N; ++i) v[i] = ld16(rs, off[i]);
    bool ok = true;
#pragma unroll
    for (int i = 0; i < N; ++i) ok = ok && ok4(v[i]);
    if (__all(ok) || spins > SPIN_CAP) break;
    sleep_n(slp);
  }
}
template <int N> DEV void pollg(rsrc_t rs, const int (&off)[N], v4u (&v)[N], u32 tag) {
  for (int spins = 0;; ++spins) {
    asm volatile("" ::: "memory");
#pragma unroll
    for (int i = 0; i < N; ++i) v[i] = ld16(rs, off[i]);
    bool ok = true;
#pragma unroll
    for (int i = 0; i < N; ++i) ok = ok && v[i].y == tag && v[i].w == tag;
    if (__all(ok) || spins > SPIN_CAP) break;
  }
}
DEV void trig4(rsrc_t rs, int off) {
  for (int spins = 0;; ++spins) {
    asm volatile("" ::: "memory");
    const u32 v = ld4(rs, off);
    if (__all(v != SENT) || spins > SPIN_CAP) break;
    __builtin_amdgcn_s_sleep(1);
  }
}
DEV void trigt(rsrc_t rs, int off, u32 tag) {
  for (int spins = 0;; ++spins) {
    asm volatile("" ::: "memory");
    const u32 v = ld4(rs, off);
    if (__all(v == tag) || spins > SPIN_CAP) break;
    __builtin_amdgcn_s_sleep(1);
  }
}
template <int N> DEV void pollgs(rsrc_t rs, const int (&off)[N], v4u (&v)[N], u32 tag, int slp) {
  for (int spins = 0;; ++spins) {
    asm volatile("" ::: "memory");
#pragma unroll
    for (int i = 0; i < N; ++i) v[i] = ld16(rs, off[i]);
    bool ok = true;
#pragma unroll
    for (int i = 0; i < N; ++i) ok = ok && v[i].y == tag && v[i].w == tag;
    if (__all(ok) || spins > SPIN_CAP) break;
    sleep_n(slp);
  }
}
DEV void poll_flags128(rsrc_t rs, int base, int lane, u32 tag) {
  for (int spins = 0;; ++spins) {
    asm volatile("" ::: "memory");
    const v4u f = ld16(rs, base + (lane & 31) * 16);
    if (__all(f.x == tag && f.y == tag && f.z == tag && f.w == tag) || spins > SPIN_CAP) break;
  }
}

#define STAMP(i)
#define STAMP_DECL
#define STAMP_FLUSH()

struct U16 { v4u w[8]; };
struct U8 { v2u w[8]; };
template <bool NT> DEV void load_u16(U16& U, const u16* base, int ld, int lane) {
  const int g = lane >> 4, n = lane & 15;
  const u16* p = base + (size_t)(32 * (n >> 2) + 8 * g) * ld + 8 * (n & 3);
#pragma unroll
  for (int i = 0; i < 8; ++i) U.w[i] = NT ? gld16nt(p + (size_t)i * ld) : gld16(p + (size_t)i * ld);
}
template <bool NT> DEV void load_u16w(U16& U, const u16* base, int ld, int lane) {
  const int g = lane >> 4, n = lane & 15;
  const u16* p = base + (size_t)(32 * (n >> 3) + 8 * g) * ld + 8 * (n & 7);
#pragma unroll
  for (int i = 0; i < 8; ++i) U.w[i] = NT ? gld16nt(p + (size_t)i * ld) : gld16(p + (size_t)i * ld);
}
template <bool NT> DEV void load_u8(U8& U, const u16* base, int ld, int lane) {
  const int g = lane >> 4, n = lane & 15;
  const u16* p = base + (size_t)(32 * (n >> 2) + 8 * g) * ld + 4 * (n & 3);
#pragma unroll
  for (int i = 0; i < 8; ++i) U.w[i] = NT ? gld8nt(p + (size_t)i * ld) : gld8(p + (size_t)i * ld);
}
DEV void tr_u16(const U16& U, v4u (&f)[8]) {
#pragma unroll
  for (int j = 0; j < 8; ++j) {
    const u32 sel = (j & 1) ? 0x07060302u : 0x05040100u;
#pragma unroll
    for (int t = 0; t < 4; ++t) f[j][t] = __builtin_amdgcn_perm(U.w[2 * t + 1][j >> 1], U.w[2 * t][j >> 1], sel);
  }
}
DEV void tr_u8(const U8& U, v4u (&f)[4]) {
#pragma unroll
  for (int j = 0; j < 4; ++j) {
    const u32 sel = (j & 1) ? 0x07060302u : 0x05040100u;
#pragma unroll
    for (int t = 0; t < 4; ++t) f[j][t] = __builtin_amdgcn_perm(U.w[2 * t + 1][j >> 1], U.w[2 * t][j >> 1], sel);
  }
}
DEV v4u silu_frag(v4u cv) {
  v4u o;
#pragma unroll
  for (int t = 0; t < 4; ++t) o[t] = pk2(silu(bflo(cv[t])), silu(bfhi(cv[t])));
  return o;
}

DEV int fsw(int row) { return ((row & 3) << 2) | ((row >> 2) & 3); }
constexpr int RS = 288;

DEV void ch_role(const Params& p, LAS unsigned char* L, rsrc_t rs, int x, int r, int tid, int lane, int w) {
  STAMP_DECL
  STAMP(0);
  const int h = 4 * x + (r >> 2), j = r & 3, dh = j & 1, kh = j >> 1;
  const int chi = 16 * x + r;
  const bool isA = r < 5;
  const int ia = 5 * x + r, slA = ia % 10, matA = (ia / 10) & 1, khA = ia / 20;
  const int g = lane >> 4, n = lane & 15;
  const int bp = j & 1, chf = j >> 1;
  const bool isD3 = r < 10;
  const int id3 = 10 * x + r, ks3 = id3 / 10, slab3 = id3 % 10;
  const bool isM = r >= 10 && r < 15;
  const int slabM = 5 * x + (r - 10);
  constexpr int UKS = 0;
  constexpr int RED_A = 32768, RED_Q = RED_A + 10 * RS * 4, RED_D = RED_Q + 20 * RS * 4, RED_3 = RED_D + 32 * RS * 4, STG = RED_3 + 32 * RS * 4, XST = STG + 2048, QST = XST + 128, RED_F = QST + 2048;
  static_assert(RED_F + 2048 <= LDS_BYTES, "lds");
  LAS float* redA = (LAS float*)(L + RED_A);
  LAS float* redQ = (LAS float*)(L + RED_Q);
  LAS float* redD = (LAS float*)(L + RED_D);
  LAS float* red3 = (LAS float*)(L + RED_3);
  LAS float* xst = (LAS float*)(L + XST);
  const v4u S4 = {SENT, SENT, SENT, SENT};
  const GAS u32* epp = (const GAS u32*)(p.ws + OFF_EPOCH);

  auto uk_dma = [&](int i0, int i1) {
#pragma unroll
    for (int i = i0; i < i1; ++i) {
      const int row = 4 * i + (lane >> 4);
      const int ch = (lane & 15) ^ fsw(row);
      dma16_nt(p.wuk + ((size_t)(128 * j + row) * 32 + h) * 128 + ch * 8, UKS + 4 * i * 256);
    }
    SCHED();
  };

  U16 uq;
  u32 ep;
  v4u fq[8];
  if (w < 5) {
    if (isA) {
      U16 ua;
      const v4u cfr = gld16(p.c + (n & 3) * 640 + 320 * khA + 64 * w + 32 * ((n >> 2) & 1) + 8 * g);
      load_u16w<true>(ua, p.wmod + (size_t)(320 * khA + 64 * w) * 3840 + 640 * matA + 64 * slA, 3840, lane);
      SCHED();
      const v4u af = silu_frag(cfr);
      v4u fa[8];
      tr_u16(ua, fa);
      STAMP(19);
      f32x4 acc[8];
#pragma unroll
      for (int q = 0; q < 8; ++q) acc[q] = mfma16(af, fa[q], f32x4{0, 0, 0, 0});
      if (g < 2 && (n >> 3) == g) {
#pragma unroll
        for (int b = 0; b < 4; ++b) {
          *(LAS f32x4*)(redA + (w * 2 + g) * RS + b * 64 + 8 * (n & 7)) = f32x4{acc[0][b], acc[1][b], acc[2][b], acc[3][b]};
          *(LAS f32x4*)(redA + (w * 2 + g) * RS + b * 64 + 8 * (n & 7) + 4) = f32x4{acc[4][b], acc[5][b], acc[6][b], acc[7][b]};
        }
      }
      STAMP(1);
      BAR();
      BAR();
    } else __builtin_amdgcn_s_sleep(CH_DELAY);
    load_u16w<true>(uq, p.wq + (size_t)(320 * kh + 64 * w) * 4096 + h * 128 + 64 * dh, 4096, lane);
    ep = __hip_atomic_load(epp, __ATOMIC_RELAXED, __HIP_MEMORY_SCOPE_AGENT);
    SCHED();
  } else if (w == 7) {
    ep = __hip_atomic_load(epp, __ATOMIC_RELAXED, __HIP_MEMORY_SCOPE_AGENT);
    asm volatile("" ::: "memory");
    st16(rs, OFF_QABS + (lane >> 4) * 32768 + h * 1024 + j * 256 + (lane & 15) * 16, S4);
    if (lane < 32) st16(rs, OFF_CTX + chf * 32768 + (2 * bp + (lane >> 4)) * 8192 + h * 256 + (lane & 15) * 16, S4);
    else if (lane < 40) st16(rs, OFF_STATS + chi * 128 + (lane - 32) * 16, S4);
    if (lane < 32) { if (chi < 80) st16(rs, OFF_ATTN + chi * 512 + lane * 16, S4); }
    else { if (chi < 40) st16(rs, OFF_MODR + chi * 512 + (lane - 32) * 16, S4); }
    if (chi < 108) {
#pragma unroll
      for (int i = 0; i < 5; ++i) st16(rs, OFF_OUTP + ((8 * i + (lane >> 3)) * 108 + chi) * 128 + (lane & 7) * 16, S4);
    }
    STAMP(1);
    if (isA) { BAR(); BAR(); }
    DRAIN();
    if (lane == 0) st4(rs, OFF_FLAG1 + chi * 4, ep + 1u);
    STAMP(2);
  } else if (w == 6) {
    ep = 0;
    if (isA) {
      u32 d[4][5];
#pragma unroll
      for (int b = 0; b < 4; ++b)
#pragma unroll
        for (int i = 0; i < 5; ++i) d[b][i] = *(const GAS u32*)(p.x + b * 640 + 2 * (lane + 64 * i));
      SCHED();
#pragma unroll
      for (int b = 0; b < 4; ++b) {
        float s = 0.f;
#pragma unroll
        for (int i = 0; i < 5; ++i) s += bflo(d[b][i]) + bfhi(d[b][i]);
        const float m = wave_sum(s) * (1.f / 640.f);
        float q2 = 0.f;
#pragma unroll
        for (int i = 0; i < 5; ++i) { const float d0 = bflo(d[b][i]) - m, d1 = bfhi(d[b][i]) - m; q2 += d0 * d0 + d1 * d1; }
        const float var = wave_sum(q2) * (1.f / 640.f);
        if (lane == 0) { xst[2 * b] = m; xst[2 * b + 1] = rsqrtf(var + 1e-5f); }
      }
      STAMP(1);
      BAR(); BAR();
    } else __builtin_amdgcn_s_sleep(CH_DELAY);
    uk_dma(0, 32);
  } else {
    ep = __hip_atomic_load(epp, __ATOMIC_RELAXED, __HIP_MEMORY_SCOPE_AGENT);
    if (isA) {
      const v2u xa2 = gld8(p.x + (lane >> 4) * 640 + 64 * slA + 4 * (lane & 15));
      BAR();
      f32x4 v = reduce256w<10, RS>(redA, lane);
      if (matA) {
        const float m = xst[2 * (lane >> 4)], rstd = xst[2 * (lane >> 4) + 1], one = khA == 0 ? 1.f : 0.f;
        v[0] = (bflo(xa2.x) - m) * rstd * (v[0] + one); v[1] = (bfhi(xa2.x) - m) * rstd * (v[1] + one);
        v[2] = (bflo(xa2.y) - m) * rstd * (v[2] + one); v[3] = (bfhi(xa2.y) - m) * rstd * (v[3] + one);
      }
      st16(rs, OFF_H + ((matA + 2 * khA) * 10 + slA) * 1024 + lane * 16, v4u{pk2(v[0], v[1]), ep + 1u, pk2(v[2], v[3]), ep + 1u});
      STAMP(2);
      BAR();
    }
  }
  const u32 tag = ep + 1u;
  if (w < 5) {
    tr_u16(uq, fq);
#pragma unroll
    for (int q = 0; q < 8; ++q) PIN4(fq[q]);
    STAMP(3);
    const int o = OFF_H + ((n >> 3) * 10 + 5 * kh + w) * 1024 + (n & 3) * 256 + 128 * ((n >> 2) & 1) + 32 * g;
    int off[4] = {o, o + 16, o + 20480, o + 20480 + 16};
    v4u a[4];
    pollgs<4>(rs, off, a, tag, HSLP);
    STAMP(4);
    const v4u af0 = {a[0].x, a[0].z, a[1].x, a[1].z}, af1 = {a[2].x, a[2].z, a[3].x, a[3].z};
    f32x4 acc[8];
#pragma unroll
    for (int q = 0; q < 8; ++q) { acc[q] = mfma16(af0, fq[q], f32x4{0, 0, 0, 0}); acc[q] = mfma16(af1, fq[q], acc[q]); }
    if ((n >> 3) == (g & 1)) {
#pragma unroll
      for (int b = 0; b < 4; ++b) {
        *(LAS f32x4*)(redQ + (w * 4 + g) * RS + b * 64 + 8 * (n & 7)) = f32x4{acc[0][b], acc[1][b], acc[2][b], acc[3][b]};
        *(LAS f32x4*)(redQ + (w * 4 + g) * RS + b * 64 + 8 * (n & 7) + 4) = f32x4{acc[4][b], acc[5][b], acc[6][b], acc[7][b]};
      }
    }
  }
  BAR();
  STAMP(5);
  if (w < 6 || w == 7) {
  }
  {
    if (w == 6) ep = __hip_atomic_load(epp, __ATOMIC_RELAXED, __HIP_MEMORY_SCOPE_AGENT);
    const u32 tg = ep + 1u;
    const f32x4 v = reduce256<20, RS>(redQ, w, lane);
    if (lane < 8) st16(rs, OFF_Q + (h * 4 + j) * 1024 + (8 * w + lane) * 16, v4u{pk2(v[0], v[1]), tg, pk2(v[2], v[3]), tg});
  }
  STAMP(6);
  {
    if (w < 4) {
      int off[1] = {OFF_Q + h * 4096 + tid * 16}; v4u a[1];
      sleep_n(QDELAY);
      pollg<1>(rs, off, a, tag);
      *(LAS v2u*)(L + QST + tid * 8) = v2u{a[0].x, a[0].z};
    } else if (w == 6) DRAIN();
    BAR();
    STAMP(7);
    f32x4 acc = {0, 0, 0, 0};
    const int row = 16 * w + n;
    v4u af[4], bfr[4];
#pragma unroll
    for (int t = 0; t < 4; ++t) {
      af[t] = *(LAS v4u*)(L + QST + ((((n >> 2) & 1) << 1) + (t >> 1)) * 512 + (n & 3) * 128 + (32 * (t & 1) + 8 * g) * 2);
      bfr[t] = *(LAS v4u*)(L + UKS + row * 256 + (((4 * t + g) ^ fsw(row)) << 4));
    }
#pragma unroll
    for (int t = 0; t < 4; ++t) acc = mfma16(af[t], bfr[t], acc);
    float v;
    {
      const float s0 = x16_sum(acc[0]), s1 = x16_sum(acc[1]), s2 = x16_sum(acc[2]), s3 = x16_sum(acc[3]);
      v = (g == 0 ? s0 : g == 1 ? s1 : g == 2 ? s2 : s3) * (0.08838834764831845f * 1.4426950408889634f);
    }
    LAS u16* stg = (LAS u16*)(L + STG);
    stg[g * 128 + 16 * w + n] = (u16)(pk2(v, 0.f) & 0xFFFFu);
  }
  BAR();
  if (w == 0) {
    const v4u v = *(LAS v4u*)(L + STG + lane * 16);
    st16(rs, OFF_QABS + (lane >> 4) * 32768 + h * 1024 + j * 256 + (lane & 15) * 16, san4(v));
  }
  BAR();
  STAMP(8);
  U16 uv, uo, um[2]; v4u cf[2];
  {
    const u16* pv = p.wuv + ((size_t)(256 * chf + 32 * w + 8 * g) * 32 + h) * 128 + 8 * n;
#pragma unroll
    for (int i = 0; i < 8; ++i) uv.w[i] = gld16nt(pv + (size_t)i * 4096);
    if (isD3) load_u16w<true>(uo, p.wo + (size_t)(512 * ks3 + 64 * w) * 640 + 64 * slab3, 640, lane);
    if (isM && w < 5) {
#pragma unroll
      for (int u = 0; u < 2; ++u) {
        cf[u] = gld16(p.c + (n & 3) * 640 + 128 * w + 64 * u + 32 * ((n >> 2) & 1) + 8 * g);
        load_u16w<true>(um[u], p.wmod + (size_t)(128 * w + 64 * u) * 3840 + 1280 + 64 * slabM, 3840, lane);
      }
    }
    SCHED();
  }
  STAMP(9);
  if (w == 7) poll_flags128(rs, OFF_FLAG1, lane, tag);
  v4u fv[8], fo[8];
  tr_u16(uv, fv);
#pragma unroll
  for (int q = 0; q < 8; ++q) PIN4(fv[q]);
  if (isD3) {
    tr_u16(uo, fo);
#pragma unroll
    for (int q = 0; q < 8; ++q) PIN4(fo[q]);
  }
  if (isM && w < 5) {
    f32x4 acc[8];
#pragma unroll
    for (int q = 0; q < 8; ++q) acc[q] = f32x4{0, 0, 0, 0};
#pragma unroll
    for (int u = 0; u < 2; ++u) {
      v4u fm[8];
      tr_u16(um[u], fm);
      const v4u af = silu_frag(cf[u]);
#pragma unroll
      for (int q = 0; q < 8; ++q) acc[q] = mfma16(af, fm[q], acc[q]);
    }
    if (g < 2 && (n >> 3) == g) {
#pragma unroll
      for (int bb = 0; bb < 4; ++bb) {
        *(LAS f32x4*)(red3 + (w * 2 + g) * RS + bb * 64 + 8 * (n & 7)) = f32x4{acc[0][bb], acc[1][bb], acc[2][bb], acc[3][bb]};
        *(LAS f32x4*)(red3 + (w * 2 + g) * RS + bb * 64 + 8 * (n & 7) + 4) = f32x4{acc[4][bb], acc[5][bb], acc[6][bb], acc[7][bb]};
      }
    }
  }
  BAR();
  if (isM && w < 4) {
    const f32x4 v = reduce256q<10, RS>(red3, w, lane);
    const v4u o = pack8_pairs(v);
    if (lane < 16 && !(lane & 1)) st16(rs, OFF_MODR + slabM * 512 + (16 * w + lane) * 8, san4(o));
  }
  STAMP(10);
  {
    int off[4]; v4u a[4];
#pragma unroll
    for (int mt = 0; mt < 4; ++mt) off[mt] = OFF_O + (((2 * bp + (mt >> 1)) * 32 + h) * 2 + chf) * 16384 + (16 * (mt & 1) + n) * 512 + 64 * w + 16 * g;
    const int so = OFF_STATS + ((2 * bp + (lane >> 5)) * 32 + (lane & 31)) * 128 + h * 4;
    if (w == 7) trig4(rs, so);
    BAR();
    STAMP(11);
    const u32 lse_u = ld4(rs, so);
#pragma unroll
    for (int mt = 0; mt < 4; ++mt) a[mt] = ld16(rs, off[mt]);
    SCHED();
    const float lse = u2f(lse_u);
    const float mx = x16_max(row_max(lse));
    const float ex = __builtin_amdgcn_exp2f(lse - mx);
    const float al = ex / x16_sum(row_sum(ex));
    float wt[2][8];
#pragma unroll
    for (int bb = 0; bb < 2; ++bb)
#pragma unroll
      for (int mt = 0; mt < 2; ++mt)
#pragma unroll
        for (int i = 0; i < 4; ++i) wt[bb][mt * 4 + i] = __shfl(al, 32 * bb + 16 * mt + 4 * g + i);
#pragma unroll
    for (int bb = 0; bb < 2; ++bb) {
      f32x4 acc[2][8];
#pragma unroll
      for (int mt = 0; mt < 2; ++mt)
#pragma unroll
        for (int q = 0; q < 8; ++q) acc[mt][q] = mfma16(a[2 * bb + mt], fv[q], f32x4{0, 0, 0, 0});
      if (bb == 0) STAMP(16);
      const f32x4 w0 = {wt[bb][0], wt[bb][1], wt[bb][2], wt[bb][3]}, w1 = {wt[bb][4], wt[bb][5], wt[bb][6], wt[bb][7]};
      float res[8];
#pragma unroll
      for (int q = 0; q < 8; ++q) { const f32x4 t = acc[0][q] * w0 + acc[1][q] * w1; res[q] = (t[0] + t[1]) + (t[2] + t[3]); }
      *(LAS f32x4*)(redD + (w * 4 + g) * RS + bb * 128 + 8 * n) = f32x4{res[0], res[1], res[2], res[3]};
      *(LAS f32x4*)(redD + (w * 4 + g) * RS + bb * 128 + 8 * n + 4) = f32x4{res[4], res[5], res[6], res[7]};
    }
  }
  STAMP(17);
  BAR();
  if (w < 4) {
    const f32x4 v = reduce256q<32, RS>(redD, w, lane);
    const v4u o = pack8_pairs(v);
    const int G2 = 16 * w + (lane & 15);
    if (lane < 16 && !(lane & 1)) st16(rs, OFF_CTX + chf * 32768 + (2 * bp + (G2 >> 5)) * 8192 + h * 256 + (G2 & 31) * 8, san4(o));
  }
  STAMP(12);
  if (isD3) {
    const int k0 = 512 * ks3 + 64 * w + 32 * ((n >> 2) & 1) + 8 * g;
    int off[1] = {OFF_CTX + (n >> 3) * 32768 + (n & 3) * 8192 + k0 * 2};
    v4u a[1];
    sleep_n(CDELAY);
    poll16s<1>(rs, off, a, 1);
    STAMP(13);
    f32x4 acc[8];
#pragma unroll
    for (int q = 0; q < 8; ++q) acc[q] = mfma16(a[0], fo[q], f32x4{0, 0, 0, 0});
    if ((n >> 3) == (g & 1)) {
#pragma unroll
      for (int bb = 0; bb < 4; ++bb) {
        *(LAS f32x4*)(red3 + (w * 4 + g) * RS + bb * 64 + 8 * (n & 7)) = f32x4{acc[0][bb], acc[1][bb], acc[2][bb], acc[3][bb]};
        *(LAS f32x4*)(red3 + (w * 4 + g) * RS + bb * 64 + 8 * (n & 7) + 4) = f32x4{acc[4][bb], acc[5][bb], acc[6][bb], acc[7][bb]};
      }
    }
    BAR();
    if (w < 4) {
      const f32x4 v = reduce256q<32, RS>(red3, w, lane);
      const v4u o = pack8_pairs(v);
      if (lane < 16 && !(lane & 1)) st16(rs, OFF_ATTN + id3 * 512 + (16 * w + lane) * 8, san4(o));
    }
    STAMP(14);
  }
  if (isM) {
    float x1 = 0.f, gbv = 0.f;
    if (w == 0) {
      const int bb = lane >> 4, col = 16 * slabM + (lane & 15);
      const int sub = bb * 128 + ((col & 63) * 2 & ~3);
      const u32 xv = (u32)p.x[bb * 640 + col];
      u32 v[10];
      for (int spins = 0;; ++spins) {
        asm volatile("" ::: "memory");
#pragma unroll
        for (int k = 0; k < 8; ++k) v[k] = ld4(rs, OFF_ATTN + (k * 10 + (col >> 6)) * 512 + sub);
        v[8] = ld4(rs, OFF_MODR + (0 * 10 + (col >> 6)) * 512 + sub);
        v[9] = ld4(rs, OFF_MODR + (3 * 10 + (col >> 6)) * 512 + sub);
        bool ok = true;
#pragma unroll
        for (int k = 0; k < 10; ++k) ok = ok && v[k] != SENT;
        if (__all(ok) || spins > SPIN_CAP) break;
        __builtin_amdgcn_s_sleep(4);
      }
      if (slabM == 0 && lane == 0) st4(rs, OFF_EPOCH, tag);
      float f[10], at = 0.f;
#pragma unroll
      for (int k = 0; k < 10; ++k) f[k] = (col & 1) ? bfhi(v[k]) : bflo(v[k]);
#pragma unroll
      for (int k = 0; k < 8; ++k) at += f[k];
      x1 = bflo(xv) + f[8] * at;
      gbv = f[9];
    }
    BAR();
    STAMP(13);
    const int e0 = tid >> 3, e1 = (64 + e0 < 108) ? 64 + e0 : e0;
    int off[2] = {OFF_OUTP + (slabM * 108 + e0) * 128 + (tid & 7) * 16, OFF_OUTP + (slabM * 108 + e1) * 128 + (tid & 7) * 16};
    v4u a[2];
    if (w >= 6) { const int t2 = tid - 384; trig4(rs, OFF_OUTP + (slabM * 108 + (t2 < 108 ? t2 : 107)) * 128 + 124); }
    BAR();
    poll16<2>(rs, off, a);
    STAMP(14);
    float v8[8];
    const float m1 = (64 + e0 < 108) ? 1.f : 0.f;
#pragma unroll
    for (int t = 0; t < 4; ++t) { v8[2 * t] = bflo(a[0][t]) + m1 * bflo(a[1][t]); v8[2 * t + 1] = bfhi(a[0][t]) + m1 * bfhi(a[1][t]); }
#pragma unroll
    for (int t = 0; t < 8; ++t) { float v = v8[t]; v += dppf<0x128>(v); v = x32_sum(x16_sum(v)); v8[t] = v; }
    LAS float* redf = (LAS float*)(L + RED_F);
    if (lane < 8) {
      *(LAS f32x4*)(redf + w * 64 + lane * 8) = f32x4{v8[0], v8[1], v8[2], v8[3]};
      *(LAS f32x4*)(redf + w * 64 + lane * 8 + 4) = f32x4{v8[4], v8[5], v8[6], v8[7]};
    }
    BAR();
    if (w == 0) {
      float y = 0.f;
#pragma unroll
      for (int i = 0; i < 8; ++i) y += redf[i * 64 + lane];
      const float o = x1 + gbv * y;
      p.out[(lane >> 4) * 640 + 16 * slabM + (lane & 15)] = (u16)(pk2(o, 0.f) & 0xFFFFu);
    }
    STAMP(15);
  }
  STAMP_FLUSH();
}

DEV void att_role(const Params& p, LAS unsigned char* L, rsrc_t rs, int x, int r, int tid, int lane, int w) {
  STAMP_DECL
  STAMP(0);
  const int b = x >> 1, s = ((x & 1) << 4) | r;
  const int e = 14 * x + r;
  const bool isE = r < 14 && e < 108;
  const int g = lane >> 4, n = lane & 15, hh = lane >> 5, m32 = lane & 31;
  constexpr int QS = 131072;
  __builtin_amdgcn_s_sleep(ATT_DELAY);
  {
    const u16* cb = p.ckv + ((size_t)b * 4096 + s * 128 + 16 * w) * 512;
#pragma unroll
    for (int i = 0; i < 16; ++i) {
      const int row = 16 * w + i;
      const int ch = (lane & ~15) | ((lane & 15) ^ fsw(row));
      dma16_nt(cb + (size_t)i * 512 + ch * 8, row * 1024);
    }
  }
  const u32 tag = __hip_atomic_load((GAS u32*)(p.ws + OFF_EPOCH), __ATOMIC_RELAXED, __HIP_MEMORY_SCOPE_AGENT) + 1u;
  STAMP(1);
  if (w == 7) poll_flags128(rs, OFF_FLAG1, lane, tag);
  DRAIN();
  BAR();
  STAMP(2);
  v4u ka[32];
  if (w < 4) {
    const int row = 32 * w + m32;
    const int a0 = row * 1024 + ((hh ^ fsw(row)) << 4);
#pragma unroll
    for (int t = 0; t < 32; ++t) ka[t] = *(LAS v4u*)(L + ((a0 ^ ((t & 7) << 5)) + (t >> 3) * 256));
  }
  v4u pa[2][8];
  const int tq = (lane & 15) >> 2, tp = lane & 3, tcb = g & 1;
  auto read_pa = [&]() {
#pragma unroll
    for (int mt = 0; mt < 2; ++mt)
#pragma unroll
      for (int ks = 0; ks < 8; ++ks)
#pragma unroll
        for (int t2 = 0; t2 < 2; ++t2) {
          const int row = 16 * ks + 8 * t2 + 4 * hh + tq;
          const int ch = 8 * w + 4 * mt + 2 * tcb + (tp >> 1);
          const int f = (tq << 2) | ((2 * t2 + hh) & 3);
          const int addr = row * 1024 + ((ch ^ f) << 4) + 8 * (tp & 1);
          const s16x4 v = __builtin_amdgcn_ds_read_tr16_b64_v4i16((LAS s16x4*)(L + addr));
          const v2u u = __builtin_bit_cast(v2u, v);
          pa[mt][ks][2 * t2] = u.x; pa[mt][ks][2 * t2 + 1] = u.y;
        }
  };
  {
    int off[4]; v4u a[4];
#pragma unroll
    for (int i = 0; i < 4; ++i) off[i] = OFF_QABS + b * 32768 + (tid + 512 * i) * 16;
    if (w >= 4) read_pa();
    STAMP(3);
    if (w >= 6) trig4(rs, OFF_QABS + b * 32768 + (tid - 384) * 256 + 252);
    BAR();
    STAMP(15);
    poll16<4>(rs, off, a);
    STAMP(4);
#pragma unroll
    for (int i = 0; i < 4; ++i) {
      const int head = w + 8 * i, ch = lane;
      *(LAS v4u*)(L + QS + ch * 512 + ((head ^ (ch & 7)) << 4)) = a[i];
    }
  }
  BAR();
  STAMP(5);
  auto scr = [&](int off) { return (off >> 9) * 1024 + 512 + (off & 511); };
  constexpr int SC_P = 0, SC_MX = 8192, SC_LS = 8704;
  f32x16 sacc;
  if (w < 4) {
#pragma unroll
    for (int i = 0; i < 16; ++i) sacc[i] = 0.f;
    auto rdq = [&](int t) { const int ch = 2 * t + hh; return *(LAS v4u*)(L + QS + ch * 512 + ((m32 ^ (ch & 7)) << 4)); };
    v4u qb[8];
#pragma unroll
    for (int t = 0; t < 8; ++t) qb[t] = rdq(t);
    SCHED();
#pragma unroll
    for (int t = 0; t < 32; ++t) {
      const v4u cur = qb[t & 7];
      sacc = mfma32(ka[t], cur, sacc);
      if (t + 8 < 32) qb[t & 7] = rdq(t + 8);
      SCHED();
    }
    float mx = sacc[0];
#pragma unroll
    for (int i = 1; i < 16; ++i) mx = fmaxf(mx, sacc[i]);
    mx = x32_max(mx);
    if (hh == 0) *(LAS float*)(L + scr(SC_MX + (w * 32 + m32) * 4)) = mx;
    STAMP(6);
  }
  BAR();
  STAMP(7);
  float M;
  {
    float m0 = *(LAS float*)(L + scr(SC_MX + (0 * 32 + m32) * 4)), m1 = *(LAS float*)(L + scr(SC_MX + (1 * 32 + m32) * 4));
    float m2 = *(LAS float*)(L + scr(SC_MX + (2 * 32 + m32) * 4)), m3 = *(LAS float*)(L + scr(SC_MX + (3 * 32 + m32) * 4));
    M = fmaxf(fmaxf(m0, m1), fmaxf(m2, m3));
  }
  if (w < 4) {
    float pr[16], ls = 0.f;
#pragma unroll
    for (int i = 0; i < 16; ++i) { pr[i] = __builtin_amdgcn_exp2f(sacc[i] - M); ls += pr[i]; }
    ls = x32_sum(ls);
    if (hh == 0) *(LAS float*)(L + scr(SC_LS + (w * 32 + m32) * 4)) = ls;
#pragma unroll
    for (int sp = 0; sp < 2; ++sp) {
      v4u pf;
#pragma unroll
      for (int t = 0; t < 4; ++t) pf[t] = pk2(pr[8 * sp + 2 * t], pr[8 * sp + 2 * t + 1]);
      *(LAS v4u*)(L + scr(SC_P + ((2 * w + sp) * 64 + lane) * 16)) = pf;
    }
    read_pa();
  }
  BAR();
  STAMP(8);
  float lsum;
  {
    float l0 = *(LAS float*)(L + scr(SC_LS + (0 * 32 + m32) * 4)), l1 = *(LAS float*)(L + scr(SC_LS + (1 * 32 + m32) * 4));
    float l2 = *(LAS float*)(L + scr(SC_LS + (2 * 32 + m32) * 4)), l3 = *(LAS float*)(L + scr(SC_LS + (3 * 32 + m32) * 4));
    lsum = (l0 + l1) + (l2 + l3);
  }
  f32x16 oacc[2];
#pragma unroll
  for (int mt = 0; mt < 2; ++mt)
#pragma unroll
    for (int i = 0; i < 16; ++i) oacc[mt][i] = 0.f;
  {
    v4u pf[8];
#pragma unroll
    for (int ks = 0; ks < 8; ++ks) pf[ks] = *(LAS v4u*)(L + scr(SC_P + (ks * 64 + lane) * 16));
    SCHED();
#pragma unroll
    for (int ks = 0; ks < 8; ++ks) {
      oacc[0] = mfma32(pa[0][ks], pf[ks], oacc[0]);
      oacc[1] = mfma32(pa[1][ks], pf[ks], oacc[1]);
    }
  }
  STAMP(9);
  {
    const float inv = 1.f / lsum;
    LAS unsigned char* st = L + QS + w * 4096;
#pragma unroll
    for (int mt = 0; mt < 2; ++mt)
#pragma unroll
      for (int qd = 0; qd < 4; ++qd) {
        v2u o;
        o.x = pk2(oacc[mt][4 * qd] * inv, oacc[mt][4 * qd + 1] * inv);
        o.y = pk2(oacc[mt][4 * qd + 2] * inv, oacc[mt][4 * qd + 3] * inv);
        *(LAS v2u*)(st + m32 * 128 + (((4 * mt + qd) ^ (m32 & 7)) << 4) + 8 * hh) = o;
      }
    LGKM0();
#pragma unroll
    for (int i = 0; i < 4; ++i) {
      const int head = 8 * i + (lane >> 3), ch = lane & 7;
      const v4u v = *(LAS v4u*)(st + head * 128 + ((ch ^ (head & 7)) << 4));
      st16(rs, OFF_O + ((b * 32 + head) * 2 + (w >> 2)) * 16384 + s * 512 + (w & 3) * 128 + ch * 16, v);
    }
    STAMP(16);
    DRAIN();
    BAR();
    if (w == 0) {
      const float lse = M + __log2f(lsum);
      LAS float* sf = (LAS float*)(L + scr(SC_MX + 1024));
      if (hh == 0) sf[m32] = lse;
      LGKM0();
      if (lane < 8) { const v4u v = *(LAS v4u*)(sf + 4 * lane); st16(rs, OFF_STATS + (b * 32 + s) * 128 + lane * 16, san4(v)); }
    }
    BAR();
  }
  STAMP(10);
  constexpr int RED_E = 32768, XE = RED_E + 10240, HS = XE + 128, HID = HS + 5120, OST = HID + 512;
  if (isE) {
    LAS float* redE = (LAS float*)(L + RED_E);
    U8 ug, uu; U16 ud;
    if (w < 5) {
      load_u8<true>(ug, p.wg + (size_t)(128 * w) * 1728 + 16 * e, 1728, lane);
      load_u8<true>(uu, p.wu + (size_t)(128 * w) * 1728 + 16 * e, 1728, lane);
      SCHED();
    } else {
      const int col = 256 * (w - 5) + 128 * (g >> 1) + 8 * n;
      const u16* pd = p.wd + (size_t)(16 * e + 8 * (g & 1)) * 640 + (col < 640 ? col : 0);
#pragma unroll
      for (int i = 0; i < 8; ++i) ud.w[i] = gld16nt(pd + (size_t)i * 640);
      SCHED();
    }
    const int eb = w >> 1, ec0 = 320 * (w & 1) + 8 * (lane < 40 ? lane : 39);
    v4u mga, msb, mcb, xe;
    {
      int off[3]; v4u a[3];
#pragma unroll
      for (int k = 0; k < 3; ++k) off[k] = OFF_MODR + (k * 10 + (ec0 >> 6)) * 512 + eb * 128 + (ec0 & 63) * 2;
      xe = gld16(p.x + eb * 640 + ec0);
      poll16s<3>(rs, off, a, 2);
      mga = a[0]; msb = a[1]; mcb = a[2];
    }
    v4u fg[4], fu[4], fd[8];
    if (w < 5) {
      tr_u8(ug, fg); tr_u8(uu, fu);
#pragma unroll
      for (int q = 0; q < 4; ++q) { PIN4(fg[q]); PIN4(fu[q]); }
    } else {
      tr_u16(ud, fd);
#pragma unroll
      for (int q = 0; q < 8; ++q) PIN4(fd[q]);
    }
    STAMP(11);
    int off[8]; v4u a[8];
#pragma unroll
    for (int k = 0; k < 8; ++k) off[k] = OFF_ATTN + (k * 10 + (ec0 >> 6)) * 512 + eb * 128 + (ec0 & 63) * 2;
    if (w >= 6) { const int t2 = tid - 384; trig4(rs, OFF_ATTN + (t2 < 80 ? t2 : 79) * 512 + 508); }
    BAR();
    STAMP(12);
    poll16<8>(rs, off, a);
    STAMP(13);
    float x1[8], sm = 0.f;
#pragma unroll
    for (int t = 0; t < 4; ++t) {
      float lo = 0.f, hi = 0.f;
#pragma unroll
      for (int k = 0; k < 8; ++k) { lo += bflo(a[k][t]); hi += bfhi(a[k][t]); }
      x1[2 * t] = bflo(xe[t]) + bflo(mga[t]) * lo;
      x1[2 * t + 1] = bfhi(xe[t]) + bfhi(mga[t]) * hi;
      sm += x1[2 * t] + x1[2 * t + 1];
    }
    if (lane >= 40) sm = 0.f;
    const float mw = wave_sum(sm) * (1.f / 320.f);
    float q2 = 0.f;
#pragma unroll
    for (int i = 0; i < 8; ++i) { const float d = x1[i] - mw; q2 += d * d; }
    if (lane >= 40) q2 = 0.f;
    q2 = wave_sum(q2);
    LAS float* xs = (LAS float*)(L + XE);
    if (lane == 0) { xs[2 * w] = mw; xs[2 * w + 1] = q2; }
    BAR();
    const float mo = xs[2 * (w ^ 1)], qo = xs[2 * (w ^ 1) + 1];
    const float m = 0.5f * (mw + mo);
    const float rstd = rsqrtf((q2 + qo + 160.f * (mw - mo) * (mw - mo)) * (1.f / 640.f) + 1e-5f);
    v4u hv;
#pragma unroll
    for (int t = 0; t < 4; ++t) {
      const float lo = (x1[2 * t] - m) * rstd * (1.f + bflo(mcb[t])) + bflo(msb[t]);
      const float hi = (x1[2 * t + 1] - m) * rstd * (1.f + bfhi(mcb[t])) + bfhi(msb[t]);
      hv[t] = pk2(lo, hi);
    }
    if (lane < 40) *(LAS v4u*)(L + HS + eb * 1280 + ec0 * 2) = hv;
    BAR();
    STAMP(14);
    if (w < 5) {
      const v4u af = *(LAS v4u*)(L + HS + (n & 3) * 1280 + (128 * w + 32 * (n >> 2) + 8 * g) * 2);
      f32x4 ag[4], au[4];
#pragma unroll
      for (int q = 0; q < 4; ++q) { ag[q] = mfma16(af, fg[q], f32x4{0, 0, 0, 0}); au[q] = mfma16(af, fu[q], f32x4{0, 0, 0, 0}); }
      if ((n >> 2) == g) {
#pragma unroll
        for (int bb = 0; bb < 4; ++bb) {
          *(LAS f32x4*)(redE + (w * 4 + g) * 128 + bb * 16 + 4 * (n & 3)) = f32x4{ag[0][bb], ag[1][bb], ag[2][bb], ag[3][bb]};
          *(LAS f32x4*)(redE + (w * 4 + g) * 128 + 64 + bb * 16 + 4 * (n & 3)) = f32x4{au[0][bb], au[1][bb], au[2][bb], au[3][bb]};
        }
      }
    }
    BAR();
    STAMP(17);
    if (w >= 5) {
      f32x4 gr[5], ur[5];
#pragma unroll
      for (int k = 0; k < 5; ++k) { gr[k] = *(LAS f32x4*)(redE + (4 * k + g) * 128 + 4 * n); ur[k] = *(LAS f32x4*)(redE + (4 * k + g) * 128 + 64 + 4 * n); }
      SCHED();
      const f32x4 gg = (gr[0] + gr[1]) + (gr[2] + gr[3]) + gr[4], uu2 = (ur[0] + ur[1]) + (ur[2] + ur[3]) + ur[4];
      float hd[4];
#pragma unroll
      for (int c = 0; c < 4; ++c) {
        const float gv = x32_sum(x16_sum(gg[c])), uv2 = x32_sum(x16_sum(uu2[c]));
        const float y = 0.7978845608028654f * (gv + 0.044715f * gv * gv * gv);
        const float th = 1.f - 2.f / (__expf(2.f * y) + 1.f);
        hd[c] = 0.5f * gv * (1.f + th) * uv2;
      }
      LAS unsigned char* hb = L + HID + (w - 5) * 128;
      if (g == 0) *(LAS v2u*)(hb + n * 8) = v2u{pk2(hd[0], hd[1]), pk2(hd[2], hd[3])};
      LGKM0();
      v4u af = *(LAS v4u*)(hb + (n & 3) * 32 + (g & 1) * 16);
      if ((g >> 1) != ((n >> 2) & 1)) af = v4u{0, 0, 0, 0};
      f32x4 acc[8];
#pragma unroll
      for (int q = 0; q < 8; ++q) acc[q] = mfma16(af, fd[q], f32x4{0, 0, 0, 0});
      LAS unsigned char* ob = L + OST + (w - 5) * 2048;
      if (g < 2) {
#pragma unroll
        for (int bb = 0; bb < 4; ++bb) {
          v4u o;
#pragma unroll
          for (int t = 0; t < 4; ++t) o[t] = pk2(acc[2 * t][bb], acc[2 * t + 1][bb]);
          *(LAS v4u*)(ob + (8 * g + (n >> 1)) * 128 + bb * 32 + (n & 1) * 16) = san4(o);
        }
      }
      LGKM0();
#pragma unroll
      for (int i = 0; i < 2; ++i) {
        const int ln = 8 * i + (lane >> 3), grp = 16 * (w - 5) + ln;
        const v4u o = *(LAS v4u*)(ob + ln * 128 + (lane & 7) * 16);
        if (grp < 40) st16(rs, OFF_OUTP + (grp * 108 + e) * 128 + (lane & 7) * 16, o);
      }
    }
    STAMP(18);
  }
  STAMP_FLUSH();
}

__global__ void __launch_bounds__(512, 2) mk_kernel(Params p) {
  extern __shared__ __attribute__((aligned(16))) unsigned char smem[];
  LAS unsigned char* L = (LAS unsigned char*)smem;
  const int tid = threadIdx.x, lane = tid & 63;
  const int w = __builtin_amdgcn_readfirstlane(tid >> 6);
  const int x = blockIdx.x & 7, r = blockIdx.x >> 3;
  const rsrc_t rs = __builtin_amdgcn_make_buffer_rsrc((void*)p.ws, 0, WS_USED, 0x00020000);
  v4u cpf0 = {0, 0, 0, 0}, cpf1 = {0, 0, 0, 0};
  if (w == 5) {
    unsigned long long pc;
    asm volatile("s_getpc_b64 %0" : "=s"(pc));
    const unsigned char* base = (const unsigned char*)(pc & ~255ull);
    const int o0 = r * 1024 + lane * 16, o1 = 32768 + r * 1024 + lane * 16;
    cpf0 = *(const GAS v4u*)(base + o0);
    if (o1 + 16 <= CODE_BYTES) cpf1 = *(const GAS v4u*)(base + o1);
    asm volatile("" :: "v"(cpf0), "v"(cpf1));
  }
  if (r < 16) ch_role(p, L, rs, x, r, tid, lane, w);
  else att_role(p, L, rs, x, r - 16, tid, lane, w);
}
}

extern "C" void kernel_launch(void* const* d_in, const int* in_sizes, int n_in, void* d_out, int out_size, void* d_ws, size_t ws_size, hipStream_t stream) {
  Params p;
  p.x = (const u16*)d_in[0]; p.ckv = (const u16*)d_in[1]; p.wq = (const u16*)d_in[2]; p.wuk = (const u16*)d_in[3];
  p.wuv = (const u16*)d_in[4]; p.wo = (const u16*)d_in[5]; p.wg = (const u16*)d_in[7]; p.wu = (const u16*)d_in[8];
  p.wd = (const u16*)d_in[9]; p.c = (const u16*)d_in[10]; p.wmod = (const u16*)d_in[11];
  p.out = (u16*)d_out; p.ws = (unsigned char*)d_ws;
  (void)hipFuncSetAttribute((const void*)mk_kernel, hipFuncAttributeMaxDynamicSharedMemorySize, LDS_BYTES);
  hipLaunchKernelGGL(mk_kernel, dim3(256), dim3(512), LDS_BYTES, stream, p);
}
```

```cpp
#include <hip/hip_runtime.h>
#include <stdint.h>

namespace {
typedef unsigned short u16;
typedef unsigned u32;
typedef u32 v4u __attribute__((ext_vector_type(4)));
typedef u32 v2u __attribute__((ext_vector_type(2)));
typedef float f32x4 __attribute__((ext_vector_type(4)));
typedef float f32x16 __attribute__((ext_vector_type(16)));
typedef __bf16 bf8 __attribute__((ext_vector_type(8)));
typedef __bf16 bf2 __attribute__((ext_vector_type(2)));
typedef short s16x4 __attribute__((ext_vector_type(4)));
#define GAS __attribute__((address_space(1)))
#define LAS __attribute__((address_space(3)))
#define DEV __device__ __forceinline__

struct Params {
  const u16 *x, *ckv, *wq, *wuk, *wuv, *wo, *wg, *wu, *wd, *c, *wmod;
  u16* out;
  unsigned char* ws;
};

constexpr int OFF_EPOCH = 0;
constexpr int OFF_FLAG1 = 1024;
constexpr int OFF_FLAG2 = 2048;
constexpr int OFF_H = 4096;
constexpr int OFF_Q = 65536;
constexpr int OFF_QABS = 262144;
constexpr int OFF_STATS = 393216;
constexpr int OFF_CTX = 409600;
constexpr int OFF_ATTN = 475136;
constexpr int OFF_MODR = 516096;
constexpr int OFF_HID = 536576;
constexpr int OFF_OUTP = 557056;
constexpr int OFF_O = 2 << 20;
constexpr int WS_USED = OFF_O + (4 << 20);
constexpr int OFF_DBG = 8 << 20;

constexpr u32 SENT = 0xFFFFFFFFu;
constexpr int SPIN_CAP = 40000;
constexpr int AUX_SC1 = 16;
constexpr int LDS_BYTES = 163840;
#define TLB_WARM 0
#define CODE_PREFETCH 1
constexpr int CODE_BYTES = 33792;
#define TUNE_DECL
#define ATT_DELAY 80
#define QDELAY 2
#define HSLP 2
#define CDELAY 8
#define ATT_INFL 16
#define H_TRIG 1
#define QA_DELAY 12
#define QA_SLP 2
#define AT_DELAY 36
#define AT_SLP 0
#define OU_DELAY 35
#define UK_DELAY 16
#define CH_DELAY 8

DEV float u2f(u32 u) { return __builtin_bit_cast(float, u); }
DEV u32 f2u(float f) { return __builtin_bit_cast(u32, f); }
DEV float bflo(u32 d) { return u2f(d << 16); }
DEV float bfhi(u32 d) { return u2f(d & 0xFFFF0000u); }
DEV u32 pk2(float lo, float hi) { bf2 v = {(__bf16)lo, (__bf16)hi}; return __builtin_bit_cast(u32, v); }
DEV u32 san(u32 d) { return d < 0xFFFFFFFEu ? d : 0xFFFFFFFEu; }
DEV v4u san4(v4u v) { v.x = san(v.x); v.y = san(v.y); v.z = san(v.z); v.w = san(v.w); return v; }
DEV bool ok4(v4u v) { u32 a = v.x > v.y ? v.x : v.y, b = v.z > v.w ? v.z : v.w; return (a > b ? a : b) != SENT; }
DEV f32x4 mfma16(v4u a, v4u b, f32x4 c) { return __builtin_amdgcn_mfma_f32_16x16x32_bf16(__builtin_bit_cast(bf8, a), __builtin_bit_cast(bf8, b), c, 0, 0, 0); }
DEV f32x16 mfma32(v4u a, v4u b, f32x16 c) { return __builtin_amdgcn_mfma_f32_32x32x16_bf16(__builtin_bit_cast(bf8, a), __builtin_bit_cast(bf8, b), c, 0, 0, 0); }
template <int C> DEV float dppf(float v) { return u2f((u32)__builtin_amdgcn_update_dpp(0, (int)f2u(v), C, 0xf, 0xf, false)); }
DEV float row_sum(float v) { v += dppf<0xB1>(v); v += dppf<0x4E>(v); v += dppf<0x141>(v); v += dppf<0x140>(v); return v; }
DEV float row_max(float v) { v = fmaxf(v, dppf<0xB1>(v)); v = fmaxf(v, dppf<0x4E>(v)); v = fmaxf(v, dppf<0x141>(v)); v = fmaxf(v, dppf<0x140>(v)); return v; }
DEV float x16_sum(float v) { auto r = __builtin_amdgcn_permlane16_swap(f2u(v), f2u(v), false, false); return u2f(r[0]) + u2f(r[1]); }
DEV float x32_sum(float v) { auto r = __builtin_amdgcn_permlane32_swap(f2u(v), f2u(v), false, false); return u2f(r[0]) + u2f(r[1]); }
DEV float x16_max(float v) { auto r = __builtin_amdgcn_permlane16_swap(f2u(v), f2u(v), false, false); return fmaxf(u2f(r[0]), u2f(r[1])); }
DEV float x32_max(float v) { auto r = __builtin_amdgcn_permlane32_swap(f2u(v), f2u(v), false, false); return fmaxf(u2f(r[0]), u2f(r[1])); }
DEV float wave_sum(float v) { return x32_sum(x16_sum(row_sum(v))); }
DEV float silu(float v) { return v / (1.f + __expf(-v)); }

#define BAR() do { asm volatile("s_waitcnt lgkmcnt(0)" ::: "memory"); __builtin_amdgcn_s_barrier(); asm volatile("" ::: "memory"); } while (0)
#define DRAIN() asm volatile("s_waitcnt vmcnt(0)" ::: "memory")
#define LGKM0() asm volatile("s_waitcnt lgkmcnt(0)" ::: "memory")
#define SCHED() __builtin_amdgcn_sched_barrier(0)
#define PIN4(v) asm volatile("" : "+v"(v))

typedef __amdgpu_buffer_rsrc_t rsrc_t;
DEV v4u ld16(rsrc_t rs, int off) { return __builtin_amdgcn_raw_buffer_load_b128(rs, off, 0, AUX_SC1); }
DEV u32 ld4(rsrc_t rs, int off) { return __builtin_amdgcn_raw_buffer_load_b32(rs, off, 0, AUX_SC1); }
DEV void st16(rsrc_t rs, int off, v4u v) { __builtin_amdgcn_raw_buffer_store_b128(v, rs, off, 0, AUX_SC1); }
DEV void st4(rsrc_t rs, int off, u32 v) { __builtin_amdgcn_raw_buffer_store_b32(v, rs, off, 0, AUX_SC1); }
DEV v4u gld16(const u16* p) { return *(const GAS v4u*)p; }
DEV v2u gld8(const u16* p) { return *(const GAS v2u*)p; }
DEV v4u gld16nt(const u16* p) { return __builtin_nontemporal_load((const GAS v4u*)p); }
DEV v2u gld8nt(const u16* p) { return __builtin_nontemporal_load((const GAS v2u*)p); }

DEV void dma16_nt(const void* src, int dst) {
  u32 keep;
  asm volatile("s_mov_b32 %0, m0\n\ts_mov_b32 m0, %2\n\ts_nop 0\n\tglobal_load_lds_dwordx4 %1, off nt\n\ts_mov_b32 m0, %0" : "=&s"(keep) : "v"(src), "s"(dst) : "memory");
}
template <int NP, int RS> DEV f32x4 reduce256(LAS float* red, int w, int lane) {
  const int G = (w >> 1) * 16 + (lane & 1) * 8 + 4 * (w & 1) + ((lane >> 1) & 3), pg = lane >> 3;
  constexpr int NK = (NP + 7) / 8;
  f32x4 v[NK];
#pragma unroll
  for (int k = 0; k < NK; ++k) { const bool ok = 8 * k + 8 <= NP || pg + 8 * k < NP; v[k] = *(LAS f32x4*)(red + (ok ? pg + 8 * k : pg) * RS + 4 * G); }
  SCHED();
  f32x4 s = v[0];
#pragma unroll
  for (int k = 1; k < NK; ++k) { const bool ok = 8 * k + 8 <= NP || pg + 8 * k < NP; s += ok ? v[k] : f32x4{0, 0, 0, 0}; }
#pragma unroll
  for (int c = 0; c < 4; ++c) { float t = s[c]; t += dppf<0x128>(t); t = x32_sum(x16_sum(t)); s[c] = t; }
  return s;
}
template <int NP, int RS> DEV f32x4 reduce256w(LAS float* red, int lane) {
  f32x4 v[NP];
#pragma unroll
  for (int k = 0; k < NP; ++k) v[k] = *(LAS f32x4*)(red + k * RS + 4 * lane);
  SCHED();
#pragma unroll
  for (int st = 1; st < NP; st *= 2)
#pragma unroll
    for (int k = 0; k + st < NP; k += 2 * st) v[k] += v[k + st];
  return v[0];
}
template <int NP, int RS> DEV f32x4 reduce256q(LAS float* red, int w, int lane) {
  const int G = 16 * w + (lane & 15), pg = lane >> 4;
  constexpr int NK = (NP + 3) / 4;
  f32x4 v[NK];
#pragma unroll
  for (int k = 0; k < NK; ++k) { const bool ok = 4 * k + 4 <= NP || pg + 4 * k < NP; v[k] = *(LAS f32x4*)(red + (ok ? pg + 4 * k : pg) * RS + 4 * G); }
  SCHED();
  f32x4 s = v[0];
#pragma unroll
  for (int k = 1; k < NK; ++k) { const bool ok = 4 * k + 4 <= NP || pg + 4 * k < NP; s += ok ? v[k] : f32x4{0, 0, 0, 0}; }
#pragma unroll
  for (int c = 0; c < 4; ++c) s[c] = x32_sum(x16_sum(s[c]));
  return s;
}
DEV v4u pack8_pairs(f32x4 s) {
  const u32 a = pk2(s[0], s[1]), b = pk2(s[2], s[3]);
  const u32 na = (u32)__builtin_amdgcn_update_dpp(0, (int)a, 0xB1, 0xf, 0xf, false), nb = (u32)__builtin_amdgcn_update_dpp(0, (int)b, 0xB1, 0xf, 0xf, false);
  return v4u{a, b, na, nb};
}
DEV v4u pack8_ror(f32x4 s) {
  const u32 a = pk2(s[0], s[1]), b = pk2(s[2], s[3]);
  const u32 na = (u32)__builtin_amdgcn_update_dpp(0, (int)a, 0x128, 0xf, 0xf, false), nb = (u32)__builtin_amdgcn_update_dpp(0, (int)b, 0x128, 0xf, 0xf, false);
  return v4u{a, b, na, nb};
}
DEV void sleep_n(int n) { for (int i = 0; i < n; ++i) __builtin_amdgcn_s_sleep(1); }

template <int N> DEV void poll16(rsrc_t rs, const int (&off)[N], v4u (&v)[N]) {
  for (int spins = 0;; ++spins) {
    asm volatile("" ::: "memory");
#pragma unroll
    for (int i = 0; i < N; ++i) v[i] = ld16(rs, off[i]);
    bool ok = true;
#pragma unroll
    for (int i = 0; i < N; ++i) ok = ok && ok4(v[i]);
    if (__all(ok) || spins > SPIN_CAP) break;
  }
}
template <int N> DEV void poll16s(rsrc_t rs, const int (&off)[N], v4u (&v)[N], int slp) {
  for (int spins = 0;; ++spins) {
    asm volatile("" ::: "memory");
#pragma unroll
    for (int i = 0; i < N; ++i) v[i] = ld16(rs, off[i]);
    bool ok = true;
#pragma unroll
    for (int i = 0; i < N; ++i) ok = ok && ok4(v[i]);
    if (__all(ok) || spins > SPIN_CAP) break;
    sleep_n(slp);
  }
}
template <int N> DEV void pollg(rsrc_t rs, const int (&off)[N], v4u (&v)[N], u32 tag) {
  for (int spins = 0;; ++spins) {
    asm volatile("" ::: "memory");
#pragma unroll
    for (int i = 0; i < N; ++i) v[i] = ld16(rs, off[i]);
    bool ok = true;
#pragma unroll
    for (int i = 0; i < N; ++i) ok = ok && v[i].y == tag && v[i].w == tag;
    if (__all(ok) || spins > SPIN_CAP) break;
  }
}
DEV void trig4(rsrc_t rs, int off) {
  for (int spins = 0;; ++spins) {
    asm volatile("" ::: "memory");
    const u32 v = ld4(rs, off);
    if (__all(v != SENT) || spins > SPIN_CAP) break;
    __builtin_amdgcn_s_sleep(1);
  }
}
DEV void trig4p(rsrc_t rs, int off) {
  asm volatile("" ::: "memory");
  u32 v0 = ld4(rs, off);
  __builtin_amdgcn_s_sleep(2);
  asm volatile("" ::: "memory");
  u32 v1 = ld4(rs, off);
  __builtin_amdgcn_s_sleep(2);
  asm volatile("" ::: "memory");
  u32 v2 = ld4(rs, off);
  for (int spins = 0; spins < SPIN_CAP; ++spins) {
    if (__all(v0 != SENT)) break;
    asm volatile("" ::: "memory");
    v0 = ld4(rs, off);
    if (__all(v1 != SENT)) break;
    asm volatile("" ::: "memory");
    v1 = ld4(rs, off);
    if (__all(v2 != SENT)) break;
    asm volatile("" ::: "memory");
    v2 = ld4(rs, off);
  }
}
DEV void trigtp(rsrc_t rs, int off, u32 tag) {
  asm volatile("" ::: "memory");
  u32 v0 = ld4(rs, off);
  __builtin_amdgcn_s_sleep(2);
  asm volatile("" ::: "memory");
  u32 v1 = ld4(rs, off);
  __builtin_amdgcn_s_sleep(2);
  asm volatile("" ::: "memory");
  u32 v2 = ld4(rs, off);
  for (int spins = 0; spins < SPIN_CAP; ++spins) {
    if (__all(v0 == tag)) break;
    asm volatile("" ::: "memory");
    v0 = ld4(rs, off);
    if (__all(v1 == tag)) break;
    asm volatile("" ::: "memory");
    v1 = ld4(rs, off);
    if (__all(v2 == tag)) break;
    asm volatile("" ::: "memory");
    v2 = ld4(rs, off);
  }
}
DEV void trigt(rsrc_t rs, int off, u32 tag) {
  for (int spins = 0;; ++spins) {
    asm volatile("" ::: "memory");
    const u32 v = ld4(rs, off);
    if (__all(v == tag) || spins > SPIN_CAP) break;
    __builtin_amdgcn_s_sleep(1);
  }
}
template <int N> DEV void pollgs(rsrc_t rs, const int (&off)[N], v4u (&v)[N], u32 tag, int slp) {
  for (int spins = 0;; ++spins) {
    asm volatile("" ::: "memory");
#pragma unroll
    for (int i = 0; i < N; ++i) v[i] = ld16(rs, off[i]);
    bool ok = true;
#pragma unroll
    for (int i = 0; i < N; ++i) ok = ok && v[i].y == tag && v[i].w == tag;
    if (__all(ok) || spins > SPIN_CAP) break;
    sleep_n(slp);
  }
}
DEV void poll_flags128(rsrc_t rs, int base, int lane, u32 tag) {
  for (int spins = 0;; ++spins) {
    asm volatile("" ::: "memory");
    const v4u f = ld16(rs, base + (lane & 31) * 16);
    if (__all(f.x == tag && f.y == tag && f.z == tag && f.w == tag) || spins > SPIN_CAP) break;
  }
}

#define STAMP(i)
#define STAMP_DECL
#define STAMP_FLUSH()

struct U16 { v4u w[8]; };
struct U8 { v2u w[8]; };
template <bool NT> DEV void load_u16(U16& U, const u16* base, int ld, int lane) {
  const int g = lane >> 4, n = lane & 15;
  const u16* p = base + (size_t)(32 * (n >> 2) + 8 * g) * ld + 8 * (n & 3);
#pragma unroll
  for (int i = 0; i < 8; ++i) U.w[i] = NT ? gld16nt(p + (size_t)i * ld) : gld16(p + (size_t)i * ld);
}
template <bool NT> DEV void load_u16w(U16& U, const u16* base, int ld, int lane) {
  const int g = lane >> 4, n = lane & 15;
  const u16* p = base + (size_t)(32 * (n >> 3) + 8 * g) * ld + 8 * (n & 7);
#pragma unroll
  for (int i = 0; i < 8; ++i) U.w[i] = NT ? gld16nt(p + (size_t)i * ld) : gld16(p + (size_t)i * ld);
}
template <bool NT> DEV void load_u8(U8& U, const u16* base, int ld, int lane) {
  const int g = lane >> 4, n = lane & 15;
  const u16* p = base + (size_t)(32 * (n >> 2) + 8 * g) * ld + 4 * (n & 3);
#pragma unroll
  for (int i = 0; i < 8; ++i) U.w[i] = NT ? gld8nt(p + (size_t)i * ld) : gld8(p + (size_t)i * ld);
}
DEV void tr_u16(const U16& U, v4u (&f)[8]) {
#pragma unroll
  for (int j = 0; j < 8; ++j) {
    const u32 sel = (j & 1) ? 0x07060302u : 0x05040100u;
#pragma unroll
    for (int t = 0; t < 4; ++t) f[j][t] = __builtin_amdgcn_perm(U.w[2 * t + 1][j >> 1], U.w[2 * t][j >> 1], sel);
  }
}
DEV void tr_u8(const U8& U, v4u (&f)[4]) {
#pragma unroll
  for (int j = 0; j < 4; ++j) {
    const u32 sel = (j & 1) ? 0x07060302u : 0x05040100u;
#pragma unroll
    for (int t = 0; t < 4; ++t) f[j][t] = __builtin_amdgcn_perm(U.w[2 * t + 1][j >> 1], U.w[2 * t][j >> 1], sel);
  }
}
DEV v4u silu_frag(v4u cv) {
  v4u o;
#pragma unroll
  for (int t = 0; t < 4; ++t) o[t] = pk2(silu(bflo(cv[t])), silu(bfhi(cv[t])));
  return o;
}

DEV int fsw(int row) { return ((row & 3) << 2) | ((row >> 2) & 3); }
constexpr int RS = 288;

DEV void ch_role(const Params& p, LAS unsigned char* L, rsrc_t rs, int x, int r, int tid, int lane, int w) {
  STAMP_DECL
  STAMP(0);
  TUNE_DECL
  const int h = 4 * x + (r >> 2), j = r & 3, dh = j & 1, kh = j >> 1;
  const int chi = 16 * x + r;
  const bool isA = r < 5;
  const int ia = 5 * x + r, slA = ia % 10, matA = (ia / 10) & 1, khA = ia / 20;
  const int g = lane >> 4, n = lane & 15;
  const int bp = j & 1, chf = j >> 1;
  const bool isD3 = r < 10;
  const int id3 = 10 * x + r, ks3 = id3 / 10, slab3 = id3 % 10;
  const bool isM = r >= 10 && r < 15;
  const int slabM = 5 * x + (r - 10);
  constexpr int UKS = 0;
  constexpr int RED_A = 32768, RED_Q = RED_A + 10 * RS * 4, RED_D = RED_Q + 20 * RS * 4, RED_3 = RED_D + 32 * RS * 4, STG = RED_3 + 32 * RS * 4, XST = STG + 2048, QST = XST + 128, RED_F = QST + 2048;
  static_assert(RED_F + 2048 <= LDS_BYTES, "lds");
  LAS float* redA = (LAS float*)(L + RED_A);
  LAS float* redQ = (LAS float*)(L + RED_Q);
  LAS float* redD = (LAS float*)(L + RED_D);
  LAS float* red3 = (LAS float*)(L + RED_3);
  LAS float* xst = (LAS float*)(L + XST);
  const v4u S4 = {SENT, SENT, SENT, SENT};
  const GAS u32* epp = (const GAS u32*)(p.ws + OFF_EPOCH);

  auto uk_dma = [&](int i0, int i1) {
#pragma unroll
    for (int i = i0; i < i1; ++i) {
      const int row = 4 * i + (lane >> 4);
      const int ch = (lane & 15) ^ fsw(row);
      dma16_nt(p.wuk + ((size_t)(128 * j + row) * 32 + h) * 128 + ch * 8, UKS + 4 * i * 256);
    }
    SCHED();
  };

  U16 uq;
  u32 ep;
  v4u fq[8];
  if (w < 5) {
    if (isA) {
      U16 ua;
      const v4u cfr = gld16(p.c + (n & 3) * 640 + 320 * khA + 64 * w + 32 * ((n >> 2) & 1) + 8 * g);
      load_u16w<true>(ua, p.wmod + (size_t)(320 * khA + 64 * w) * 3840 + 640 * matA + 64 * slA, 3840, lane);
      SCHED();
      const v4u af = silu_frag(cfr);
      v4u fa[8];
      tr_u16(ua, fa);
      SCHED();
      STAMP(19);
      load_u16w<true>(uq, p.wq + (size_t)(320 * kh + 64 * w) * 4096 + h * 128 + 64 * dh, 4096, lane);
      ep = __hip_atomic_load(epp, __ATOMIC_RELAXED, __HIP_MEMORY_SCOPE_AGENT);
      SCHED();
      f32x4 acc[8];
#pragma unroll
      for (int q = 0; q < 8; ++q) acc[q] = mfma16(af, fa[q], f32x4{0, 0, 0, 0});
      if (g < 2 && (n >> 3) == g) {
#pragma unroll
        for (int b = 0; b < 4; ++b) {
          *(LAS f32x4*)(redA + (w * 2 + g) * RS + b * 64 + 4 * (n & 7)) = f32x4{acc[0][b], acc[1][b], acc[2][b], acc[3][b]};
          *(LAS f32x4*)(redA + (w * 2 + g) * RS + b * 64 + 32 + 4 * (n & 7)) = f32x4{acc[4][b], acc[5][b], acc[6][b], acc[7][b]};
        }
      }
      STAMP(1);
      BAR();
      BAR();
    } else {
      sleep_n(CH_DELAY);
      load_u16w<true>(uq, p.wq + (size_t)(320 * kh + 64 * w) * 4096 + h * 128 + 64 * dh, 4096, lane);
      ep = __hip_atomic_load(epp, __ATOMIC_RELAXED, __HIP_MEMORY_SCOPE_AGENT);
      SCHED();
    }
  } else if (w == 7) {
    ep = __hip_atomic_load(epp, __ATOMIC_RELAXED, __HIP_MEMORY_SCOPE_AGENT);
    asm volatile("" ::: "memory");
    st16(rs, OFF_QABS + (lane >> 4) * 32768 + h * 1024 + j * 256 + (lane & 15) * 16, S4);
    if (lane < 32) st16(rs, OFF_CTX + chf * 32768 + (2 * bp + (lane >> 4)) * 8192 + h * 256 + (lane & 15) * 16, S4);
    else if (lane < 40) st16(rs, OFF_STATS + chi * 128 + (lane - 32) * 16, S4);
    if (lane < 32) { if (chi < 80) st16(rs, OFF_ATTN + chi * 512 + lane * 16, S4); }
    else { if (chi < 40) st16(rs, OFF_MODR + chi * 512 + (lane - 32) * 16, S4); }
    if (chi < 108) {
#pragma unroll
      for (int i = 0; i < 5; ++i) st16(rs, OFF_OUTP + ((8 * i + (lane >> 3)) * 108 + chi) * 128 + (lane & 7) * 16, S4);
    }
    STAMP(1);
    if (isA) { BAR(); BAR(); }
    DRAIN();
    if (lane == 0) st4(rs, OFF_FLAG1 + chi * 4, ep + 1u);
    STAMP(2);
  } else if (w == 6) {
    ep = 0;
    if (isA) {
      u32 d[4][5];
#pragma unroll
      for (int b = 0; b < 4; ++b)
#pragma unroll
        for (int i = 0; i < 5; ++i) d[b][i] = *(const GAS u32*)(p.x + b * 640 + 2 * (lane + 64 * i));
      SCHED();
#pragma unroll
      for (int b = 0; b < 4; ++b) {
        float s = 0.f;
#pragma unroll
        for (int i = 0; i < 5; ++i) s += bflo(d[b][i]) + bfhi(d[b][i]);
        const float m = wave_sum(s) * (1.f / 640.f);
        float q2 = 0.f;
#pragma unroll
        for (int i = 0; i < 5; ++i) { const float d0 = bflo(d[b][i]) - m, d1 = bfhi(d[b][i]) - m; q2 += d0 * d0 + d1 * d1; }
        const float var = wave_sum(q2) * (1.f / 640.f);
        if (lane == 0) { xst[2 * b] = m; xst[2 * b + 1] = rsqrtf(var + 1e-5f); }
      }
      STAMP(1);
      BAR(); BAR();
    } else sleep_n(UK_DELAY);
    uk_dma(0, 32);
  } else {
    ep = __hip_atomic_load(epp, __ATOMIC_RELAXED, __HIP_MEMORY_SCOPE_AGENT);
    if (isA) {
      const v2u xa2 = gld8(p.x + (lane >> 4) * 640 + 64 * slA + 8 * (lane & 7) + 4 * ((lane >> 3) & 1));
      BAR();
      f32x4 v = reduce256w<10, RS>(redA, lane);
      if (matA) {
        const float m = xst[2 * (lane >> 4)], rstd = xst[2 * (lane >> 4) + 1], one = khA == 0 ? 1.f : 0.f;
        v[0] = (bflo(xa2.x) - m) * rstd * (v[0] + one); v[1] = (bfhi(xa2.x) - m) * rstd * (v[1] + one);
        v[2] = (bflo(xa2.y) - m) * rstd * (v[2] + one); v[3] = (bfhi(xa2.y) - m) * rstd * (v[3] + one);
      }
      st16(rs, OFF_H + ((matA + 2 * khA) * 10 + slA) * 1024 + (lane >> 4) * 256 + (lane & 7) * 32 + ((lane >> 3) & 1) * 16, v4u{pk2(v[0], v[1]), ep + 1u, pk2(v[2], v[3]), ep + 1u});
      STAMP(2);
      BAR();
    }
  }
  const u32 tag = ep + 1u;
  if (w < 5) {
    tr_u16(uq, fq);
#pragma unroll
    for (int q = 0; q < 8; ++q) PIN4(fq[q]);
    STAMP(3);
    const int o = OFF_H + ((n >> 3) * 10 + 5 * kh + w) * 1024 + (n & 3) * 256 + 128 * ((n >> 2) & 1) + 32 * g;
    int off[4] = {o, o + 16, o + 20480, o + 20480 + 16};
    v4u a[4];
    BAR();
    pollgs<4>(rs, off, a, tag, HSLP);
    STAMP(4);
    const v4u af0 = {a[0].x, a[0].z, a[1].x, a[1].z}, af1 = {a[2].x, a[2].z, a[3].x, a[3].z};
    f32x4 acc[8];
#pragma unroll
    for (int q = 0; q < 8; ++q) { acc[q] = mfma16(af0, fq[q], f32x4{0, 0, 0, 0}); acc[q] = mfma16(af1, fq[q], acc[q]); }
    if ((n >> 3) == (g & 1)) {
#pragma unroll
      for (int b = 0; b < 4; ++b) {
        *(LAS f32x4*)(redQ + (w * 4 + g) * RS + b * 64 + 4 * (n & 7)) = f32x4{acc[0][b], acc[1][b], acc[2][b], acc[3][b]};
        *(LAS f32x4*)(redQ + (w * 4 + g) * RS + b * 64 + 32 + 4 * (n & 7)) = f32x4{acc[4][b], acc[5][b], acc[6][b], acc[7][b]};
      }
    }
  }
  if (w >= 5) {
    if (w == 7) { const int l20 = lane < 20 ? lane : 0; trigtp(rs, OFF_H + ((l20 / 5) * 10 + 5 * kh + (l20 % 5)) * 1024 + 1020, tag); }
    BAR();
  }
  BAR();
  STAMP(5);
  if (w < 6 || w == 7) {
  }
  {
    if (w == 6) ep = __hip_atomic_load(epp, __ATOMIC_RELAXED, __HIP_MEMORY_SCOPE_AGENT);
    const u32 tg = ep + 1u;
    const f32x4 v = reduce256<20, RS>(redQ, w, lane);
    if (lane < 8) st16(rs, OFF_Q + (h * 4 + j) * 1024 + (8 * w + lane) * 16, v4u{pk2(v[0], v[1]), tg, pk2(v[2], v[3]), tg});
  }
  STAMP(6);
  {
    if (w < 4) {
      int off[1] = {OFF_Q + h * 4096 + tid * 16}; v4u a[1];
      sleep_n(QDELAY);
      pollg<1>(rs, off, a, tag);
      *(LAS v2u*)(L + QST + tid * 8) = v2u{a[0].x, a[0].z};
    } else if (w == 6) DRAIN();
    BAR();
    STAMP(7);
    f32x4 acc = {0, 0, 0, 0};
    const int row = 16 * w + n;
    v4u af[4], bfr[4];
#pragma unroll
    for (int t = 0; t < 4; ++t) {
      af[t] = *(LAS v4u*)(L + QST + ((((n >> 2) & 1) << 1) + (t >> 1)) * 512 + (n & 3) * 128 + (32 * (t & 1) + 8 * g) * 2);
      bfr[t] = *(LAS v4u*)(L + UKS + row * 256 + (((4 * t + g) ^ fsw(row)) << 4));
    }
#pragma unroll
    for (int t = 0; t < 4; ++t) acc = mfma16(af[t], bfr[t], acc);
    float v;
    {
      const float s0 = x16_sum(acc[0]), s1 = x16_sum(acc[1]), s2 = x16_sum(acc[2]), s3 = x16_sum(acc[3]);
      v = (g == 0 ? s0 : g == 1 ? s1 : g == 2 ? s2 : s3) * (0.08838834764831845f * 1.4426950408889634f);
    }
    LAS u16* stg = (LAS u16*)(L + STG);
    stg[g * 128 + 16 * w + n] = (u16)(pk2(v, 0.f) & 0xFFFFu);
  }
  BAR();
  if (w == 0) {
    const v4u v = *(LAS v4u*)(L + STG + lane * 16);
    st16(rs, OFF_QABS + (lane >> 4) * 32768 + h * 1024 + j * 256 + (lane & 15) * 16, san4(v));
  }
  BAR();
  STAMP(8);
  U16 uv, uo, um[2]; v4u cf[2];
  {
    const u16* pv = p.wuv + ((size_t)(256 * chf + 32 * w + 8 * g) * 32 + h) * 128 + 8 * n;
#pragma unroll
    for (int i = 0; i < 8; ++i) uv.w[i] = gld16nt(pv + (size_t)i * 4096);
    if (isD3) load_u16w<true>(uo, p.wo + (size_t)(512 * ks3 + 64 * w) * 640 + 64 * slab3, 640, lane);
    if (isM && w < 5) {
#pragma unroll
      for (int u = 0; u < 2; ++u) {
        cf[u] = gld16(p.c + (n & 3) * 640 + 128 * w + 64 * u + 32 * ((n >> 2) & 1) + 8 * g);
        load_u16w<true>(um[u], p.wmod + (size_t)(128 * w + 64 * u) * 3840 + 1280 + 64 * slabM, 3840, lane);
      }
    }
    SCHED();
  }
  STAMP(9);
  if (w == 7) poll_flags128(rs, OFF_FLAG1, lane, tag);
  v4u fv[8], fo[8];
  tr_u16(uv, fv);
#pragma unroll
  for (int q = 0; q < 8; ++q) PIN4(fv[q]);
  if (isD3) {
    tr_u16(uo, fo);
#pragma unroll
    for (int q = 0; q < 8; ++q) PIN4(fo[q]);
  }
  if (isM && w < 5) {
    f32x4 acc[8];
#pragma unroll
    for (int q = 0; q < 8; ++q) acc[q] = f32x4{0, 0, 0, 0};
#pragma unroll
    for (int u = 0; u < 2; ++u) {
      v4u fm[8];
      tr_u16(um[u], fm);
      const v4u af = silu_frag(cf[u]);
#pragma unroll
      for (int q = 0; q < 8; ++q) acc[q] = mfma16(af, fm[q], acc[q]);
    }
    if (g < 2 && (n >> 3) == g) {
#pragma unroll
      for (int bb = 0; bb < 4; ++bb) {
        *(LAS f32x4*)(red3 + (w * 2 + g) * RS + bb * 64 + 4 * (n & 7)) = f32x4{acc[0][bb], acc[1][bb], acc[2][bb], acc[3][bb]};
        *(LAS f32x4*)(red3 + (w * 2 + g) * RS + bb * 64 + 32 + 4 * (n & 7)) = f32x4{acc[4][bb], acc[5][bb], acc[6][bb], acc[7][bb]};
      }
    }
  }
  BAR();
  if (isM && w < 4) {
    const f32x4 v = reduce256q<10, RS>(red3, w, lane);
    const v4u o = pack8_ror(v);
    if (lane < 8) st16(rs, OFF_MODR + slabM * 512 + w * 128 + lane * 16, san4(o));
  }
  STAMP(10);
  {
    int off[4]; v4u a[4];
#pragma unroll
    for (int mt = 0; mt < 4; ++mt) off[mt] = OFF_O + (((2 * bp + (mt >> 1)) * 32 + h) * 2 + chf) * 16384 + (16 * (mt & 1) + n) * 512 + 64 * w + 16 * g;
    const int so = OFF_STATS + ((2 * bp + (lane >> 5)) * 32 + (lane & 31)) * 128 + h * 4;
    if (w == 7) trig4p(rs, so);
    BAR();
    STAMP(11);
    const u32 lse_u = ld4(rs, so);
#pragma unroll
    for (int mt = 0; mt < 4; ++mt) a[mt] = ld16(rs, off[mt]);
    SCHED();
    const float lse = u2f(lse_u);
    const float mx = x16_max(row_max(lse));
    const float ex = __builtin_amdgcn_exp2f(lse - mx);
    const float al = ex / x16_sum(row_sum(ex));
    float wt[2][8];
#pragma unroll
    for (int bb = 0; bb < 2; ++bb)
#pragma unroll
      for (int mt = 0; mt < 2; ++mt)
#pragma unroll
        for (int i = 0; i < 4; ++i) wt[bb][mt * 4 + i] = __shfl(al, 32 * bb + 16 * mt + 4 * g + i);
#pragma unroll
    for (int bb = 0; bb < 2; ++bb) {
      f32x4 acc[2][8];
#pragma unroll
      for (int mt = 0; mt < 2; ++mt)
#pragma unroll
        for (int q = 0; q < 8; ++q) acc[mt][q] = mfma16(a[2 * bb + mt], fv[q], f32x4{0, 0, 0, 0});
      if (bb == 0) STAMP(16);
      const f32x4 w0 = {wt[bb][0], wt[bb][1], wt[bb][2], wt[bb][3]}, w1 = {wt[bb][4], wt[bb][5], wt[bb][6], wt[bb][7]};
      float res[8];
#pragma unroll
      for (int q = 0; q < 8; ++q) { const f32x4 t = acc[0][q] * w0 + acc[1][q] * w1; res[q] = (t[0] + t[1]) + (t[2] + t[3]); }
      *(LAS f32x4*)(redD + (w * 4 + g) * RS + bb * 128 + 4 * n) = f32x4{res[0], res[1], res[2], res[3]};
      *(LAS f32x4*)(redD + (w * 4 + g) * RS + bb * 128 + 64 + 4 * n) = f32x4{res[4], res[5], res[6], res[7]};
    }
  }
  STAMP(17);
  BAR();
  if (w < 4) {
    const int pos = (w >> 1) * 128 + ((lane >> 3) & 1) * 64 + 4 * (8 * (w & 1) + (lane & 7)), pg = lane >> 4;
    f32x4 vv[8];
#pragma unroll
    for (int k = 0; k < 8; ++k) vv[k] = *(LAS f32x4*)(redD + (pg + 4 * k) * RS + pos);
    SCHED();
    f32x4 v = ((vv[0] + vv[1]) + (vv[2] + vv[3])) + ((vv[4] + vv[5]) + (vv[6] + vv[7]));
#pragma unroll
    for (int c = 0; c < 4; ++c) v[c] = x32_sum(x16_sum(v[c]));
    const v4u o = pack8_ror(v);
    if (lane < 8) st16(rs, OFF_CTX + chf * 32768 + (2 * bp + (w >> 1)) * 8192 + h * 256 + (w & 1) * 128 + lane * 16, san4(o));
  }
  STAMP(12);
  if (isD3) {
    const int k0 = 512 * ks3 + 64 * w + 32 * ((n >> 2) & 1) + 8 * g;
    int off[1] = {OFF_CTX + (n >> 3) * 32768 + (n & 3) * 8192 + k0 * 2};
    v4u a[1];
    sleep_n(CDELAY);
    poll16s<1>(rs, off, a, 1);
    STAMP(13);
    f32x4 acc[8];
#pragma unroll
    for (int q = 0; q < 8; ++q) acc[q] = mfma16(a[0], fo[q], f32x4{0, 0, 0, 0});
    if ((n >> 3) == (g & 1)) {
#pragma unroll
      for (int bb = 0; bb < 4; ++bb) {
        *(LAS f32x4*)(red3 + (w * 4 + g) * RS + bb * 64 + 4 * (n & 7)) = f32x4{acc[0][bb], acc[1][bb], acc[2][bb], acc[3][bb]};
        *(LAS f32x4*)(red3 + (w * 4 + g) * RS + bb * 64 + 32 + 4 * (n & 7)) = f32x4{acc[4][bb], acc[5][bb], acc[6][bb], acc[7][bb]};
      }
    }
    BAR();
    if (w < 4) {
      const f32x4 v = reduce256q<32, RS>(red3, w, lane);
      const v4u o = pack8_ror(v);
      if (lane < 8) st16(rs, OFF_ATTN + id3 * 512 + w * 128 + lane * 16, san4(o));
    }
    STAMP(14);
  }
  if (isM) {
    float x1 = 0.f, gbv = 0.f;
    if (w == 0) {
      const int bb = lane >> 4, col = 16 * slabM + (lane & 15);
      const int sub = bb * 128 + ((col & 63) * 2 & ~3);
      const u32 xv = (u32)p.x[bb * 640 + col];
      u32 v[10];
      for (int spins = 0;; ++spins) {
        asm volatile("" ::: "memory");
#pragma unroll
        for (int k = 0; k < 8; ++k) v[k] = ld4(rs, OFF_ATTN + (k * 10 + (col >> 6)) * 512 + sub);
        v[8] = ld4(rs, OFF_MODR + (0 * 10 + (col >> 6)) * 512 + sub);
        v[9] = ld4(rs, OFF_MODR + (3 * 10 + (col >> 6)) * 512 + sub);
        bool ok = true;
#pragma unroll
        for (int k = 0; k < 10; ++k) ok = ok && v[k] != SENT;
        if (__all(ok) || spins > SPIN_CAP) break;
        __builtin_amdgcn_s_sleep(4);
      }
      if (slabM == 0 && lane == 0) st4(rs, OFF_EPOCH, tag);
      float f[10], at = 0.f;
#pragma unroll
      for (int k = 0; k < 10; ++k) f[k] = (col & 1) ? bfhi(v[k]) : bflo(v[k]);
#pragma unroll
      for (int k = 0; k < 8; ++k) at += f[k];
      x1 = bflo(xv) + f[8] * at;
      gbv = f[9];
    }
    BAR();
    STAMP(13);
    const int e0 = tid >> 3, e1 = (64 + e0 < 108) ? 64 + e0 : e0;
    int off[2] = {OFF_OUTP + (slabM * 108 + e0) * 128 + (tid & 7) * 16, OFF_OUTP + (slabM * 108 + e1) * 128 + (tid & 7) * 16};
    v4u a[2];
    sleep_n(OU_DELAY);
    poll16s<2>(rs, off, a, 1);
    STAMP(14);
    float v8[8];
    const float m1 = (64 + e0 < 108) ? 1.f : 0.f;
#pragma unroll
    for (int t = 0; t < 4; ++t) { v8[2 * t] = bflo(a[0][t]) + m1 * bflo(a[1][t]); v8[2 * t + 1] = bfhi(a[0][t]) + m1 * bfhi(a[1][t]); }
#pragma unroll
    for (int t = 0; t < 8; ++t) { float v = v8[t]; v += dppf<0x128>(v); v = x32_sum(x16_sum(v)); v8[t] = v; }
    LAS float* redf = (LAS float*)(L + RED_F);
    if (lane < 8) {
      *(LAS f32x4*)(redf + w * 64 + lane * 8) = f32x4{v8[0], v8[1], v8[2], v8[3]};
      *(LAS f32x4*)(redf + w * 64 + lane * 8 + 4) = f32x4{v8[4], v8[5], v8[6], v8[7]};
    }
    BAR();
    if (w == 0) {
      float y = 0.f;
#pragma unroll
      for (int i = 0; i < 8; ++i) y += redf[i * 64 + lane];
      const float o = x1 + gbv * y;
      p.out[(lane >> 4) * 640 + 16 * slabM + (lane & 15)] = (u16)(pk2(o, 0.f) & 0xFFFFu);
    }
    STAMP(15);
  }
  STAMP_FLUSH();
}

DEV void att_role(const Params& p, LAS unsigned char* L, rsrc_t rs, int x, int r, int tid, int lane, int w) {
  STAMP_DECL
  STAMP(0);
  TUNE_DECL
  const int b = x >> 1, s = ((x & 1) << 4) | r;
  const int e = 14 * x + r;
  const bool isE = r < 14 && e < 108;
  const int g = lane >> 4, n = lane & 15, hh = lane >> 5, m32 = lane & 31;
  constexpr int QS = 131072;
  sleep_n(ATT_DELAY);
  {
    const u16* cb = p.ckv + ((size_t)b * 4096 + s * 128 + 16 * w) * 512;
#pragma unroll
    for (int i = 0; i < 16; ++i) {
      const int row = 16 * w + i;
      const int ch = (lane & ~15) | ((lane & 15) ^ fsw(row));
      dma16_nt(cb + (size_t)i * 512 + ch * 8, row * 1024);
    }
  }
  const u32 tag = __hip_atomic_load((GAS u32*)(p.ws + OFF_EPOCH), __ATOMIC_RELAXED, __HIP_MEMORY_SCOPE_AGENT) + 1u;
  STAMP(1);
  if (w == 7) poll_flags128(rs, OFF_FLAG1, lane, tag);
  DRAIN();
  BAR();
  STAMP(2);
  v4u ka[32];
  if (w < 4) {
    const int row = 32 * w + m32;
    const int a0 = row * 1024 + ((hh ^ fsw(row)) << 4);
#pragma unroll
    for (int t = 0; t < 32; ++t) ka[t] = *(LAS v4u*)(L + ((a0 ^ ((t & 7) << 5)) + (t >> 3) * 256));
  }
  v4u pa[2][8];
  const int tq = (lane & 15) >> 2, tp = lane & 3, tcb = g & 1;
  auto read_pa = [&]() {
#pragma unroll
    for (int mt = 0; mt < 2; ++mt)
#pragma unroll
      for (int ks = 0; ks < 8; ++ks)
#pragma unroll
        for (int t2 = 0; t2 < 2; ++t2) {
          const int row = 16 * ks + 8 * t2 + 4 * hh + tq;
          const int ch = 8 * w + 4 * mt + 2 * tcb + (tp >> 1);
          const int f = (tq << 2) | ((2 * t2 + hh) & 3);
          const int addr = row * 1024 + ((ch ^ f) << 4) + 8 * (tp & 1);
          const s16x4 v = __builtin_amdgcn_ds_read_tr16_b64_v4i16((LAS s16x4*)(L + addr));
          const v2u u = __builtin_bit_cast(v2u, v);
          pa[mt][ks][2 * t2] = u.x; pa[mt][ks][2 * t2 + 1] = u.y;
        }
  };
  {
    int off[4]; v4u a[4];
#pragma unroll
    for (int i = 0; i < 4; ++i) off[i] = OFF_QABS + b * 32768 + (tid + 512 * i) * 16;
    if (w >= 4) read_pa();
    STAMP(3);
    if (w >= 6) trigtp(rs, OFF_Q + (tid - 384) * 1024 + 1020, tag);
    BAR();
    STAMP(15);
    sleep_n(QA_DELAY);
    poll16s<4>(rs, off, a, QA_SLP);
    STAMP(4);
#pragma unroll
    for (int i = 0; i < 4; ++i) {
      const int head = w + 8 * i, ch = lane;
      *(LAS v4u*)(L + QS + ch * 512 + ((head ^ (ch & 7)) << 4)) = a[i];
    }
  }
  BAR();
  STAMP(5);
  auto scr = [&](int off) { return (off >> 9) * 1024 + 512 + (off & 511); };
  constexpr int SC_P = 0, SC_MX = 8192, SC_LS = 8704;
  f32x16 sacc;
  if (w < 4) {
#pragma unroll
    for (int i = 0; i < 16; ++i) sacc[i] = 0.f;
    auto rdq = [&](int t) { const int ch = 2 * t + hh; return *(LAS v4u*)(L + QS + ch * 512 + ((m32 ^ (ch & 7)) << 4)); };
    v4u qb[8];
#pragma unroll
    for (int t = 0; t < 8; ++t) qb[t] = rdq(t);
    SCHED();
#pragma unroll
    for (int t = 0; t < 32; ++t) {
      const v4u cur = qb[t & 7];
      sacc = mfma32(ka[t], cur, sacc);
      if (t + 8 < 32) qb[t & 7] = rdq(t + 8);
      SCHED();
    }
    float mx = sacc[0];
#pragma unroll
    for (int i = 1; i < 16; ++i) mx = fmaxf(mx, sacc[i]);
    mx = x32_max(mx);
    if (hh == 0) *(LAS float*)(L + scr(SC_MX + (w * 32 + m32) * 4)) = mx;
    STAMP(6);
  }
  BAR();
  STAMP(7);
  float M;
  {
    float m0 = *(LAS float*)(L + scr(SC_MX + (0 * 32 + m32) * 4)), m1 = *(LAS float*)(L + scr(SC_MX + (1 * 32 + m32) * 4));
    float m2 = *(LAS float*)(L + scr(SC_MX + (2 * 32 + m32) * 4)), m3 = *(LAS float*)(L + scr(SC_MX + (3 * 32 + m32) * 4));
    M = fmaxf(fmaxf(m0, m1), fmaxf(m2, m3));
  }
  if (w < 4) {
    float pr[16], ls = 0.f;
#pragma unroll
    for (int i = 0; i < 16; ++i) { pr[i] = __builtin_amdgcn_exp2f(sacc[i] - M); ls += pr[i]; }
    ls = x32_sum(ls);
    if (hh == 0) *(LAS float*)(L + scr(SC_LS + (w * 32 + m32) * 4)) = ls;
#pragma unroll
    for (int sp = 0; sp < 2; ++sp) {
      v4u pf;
#pragma unroll
      for (int t = 0; t < 4; ++t) pf[t] = pk2(pr[8 * sp + 2 * t], pr[8 * sp + 2 * t + 1]);
      *(LAS v4u*)(L + scr(SC_P + ((2 * w + sp) * 64 + lane) * 16)) = pf;
    }
    read_pa();
  }
  BAR();
  STAMP(8);
  float lsum;
  {
    float l0 = *(LAS float*)(L + scr(SC_LS + (0 * 32 + m32) * 4)), l1 = *(LAS float*)(L + scr(SC_LS + (1 * 32 + m32) * 4));
    float l2 = *(LAS float*)(L + scr(SC_LS + (2 * 32 + m32) * 4)), l3 = *(LAS float*)(L + scr(SC_LS + (3 * 32 + m32) * 4));
    lsum = (l0 + l1) + (l2 + l3);
  }
  f32x16 oacc[2];
#pragma unroll
  for (int mt = 0; mt < 2; ++mt)
#pragma unroll
    for (int i = 0; i < 16; ++i) oacc[mt][i] = 0.f;
  {
    v4u pf[8];
#pragma unroll
    for (int ks = 0; ks < 8; ++ks) pf[ks] = *(LAS v4u*)(L + scr(SC_P + (ks * 64 + lane) * 16));
    SCHED();
#pragma unroll
    for (int ks = 0; ks < 8; ++ks) {
      oacc[0] = mfma32(pa[0][ks], pf[ks], oacc[0]);
      oacc[1] = mfma32(pa[1][ks], pf[ks], oacc[1]);
    }
  }
  STAMP(9);
  {
    const float inv = 1.f / lsum;
    LAS unsigned char* st = L + QS + w * 4096;
#pragma unroll
    for (int mt = 0; mt < 2; ++mt)
#pragma unroll
      for (int qd = 0; qd < 4; ++qd) {
        v2u o;
        o.x = pk2(oacc[mt][4 * qd] * inv, oacc[mt][4 * qd + 1] * inv);
        o.y = pk2(oacc[mt][4 * qd + 2] * inv, oacc[mt][4 * qd + 3] * inv);
        *(LAS v2u*)(st + m32 * 128 + (((4 * mt + qd) ^ (m32 & 7)) << 4) + 8 * hh) = o;
      }
    LGKM0();
#pragma unroll
    for (int i = 0; i < 4; ++i) {
      const int head = 8 * i + (lane >> 3), ch = lane & 7;
      const v4u v = *(LAS v4u*)(st + head * 128 + ((ch ^ (head & 7)) << 4));
      st16(rs, OFF_O + ((b * 32 + head) * 2 + (w >> 2)) * 16384 + s * 512 + (w & 3) * 128 + ch * 16, v);
    }
    STAMP(16);
    DRAIN();
    BAR();
    if (w == 0) {
      const float lse = M + __log2f(lsum);
      LAS float* sf = (LAS float*)(L + scr(SC_MX + 1024));
      if (hh == 0) sf[m32] = lse;
      LGKM0();
      if (lane < 8) { const v4u v = *(LAS v4u*)(sf + 4 * lane); st16(rs, OFF_STATS + (b * 32 + s) * 128 + lane * 16, san4(v)); }
    }
    BAR();
  }
  STAMP(10);
  constexpr int RED_E = 32768, XE = RED_E + 10240, HS = XE + 128, HID = HS + 5120, OST = HID + 512;
  if (isE) {
    LAS float* redE = (LAS float*)(L + RED_E);
    U8 ug, uu; U16 ud;
    if (w < 5) {
      load_u8<false>(ug, p.wg + (size_t)(128 * w) * 1728 + 16 * e, 1728, lane);
      load_u8<false>(uu, p.wu + (size_t)(128 * w) * 1728 + 16 * e, 1728, lane);
      SCHED();
    } else {
      const int col = 256 * (w - 5) + 128 * (g >> 1) + 8 * n;
      const u16* pd = p.wd + (size_t)(16 * e + 8 * (g & 1)) * 640 + (col < 640 ? col : 0);
#pragma unroll
      for (int i = 0; i < 8; ++i) ud.w[i] = gld16nt(pd + (size_t)i * 640);
      SCHED();
    }
    const int eb = w >> 1, G0 = (w & 1) ? 3 : 0, NG = (w & 1) ? 2 : 3;
    const bool own = g < 2;
    float ga4[3][4], sb4[3][4], cb4[3][4], xx4[3][4];
    {
      u32 mv[3][3][2], xv[3][2];
      for (int spins = 0;; ++spins) {
        asm volatile("" ::: "memory");
        bool ok = true;
#pragma unroll
        for (int k = 0; k < 3; ++k) {
          const int col = 128 * (G0 + (k < NG ? k : 0)) + 8 * n + 4 * (g & 1);
#pragma unroll
          for (int kind = 0; kind < 3; ++kind) {
            const v2u t = __builtin_bit_cast(v2u, __builtin_amdgcn_raw_buffer_load_b64(rs, OFF_MODR + (kind * 10 + (col >> 6)) * 512 + eb * 128 + (col & 63) * 2, 0, AUX_SC1));
            mv[k][kind][0] = t.x; mv[k][kind][1] = t.y;
            ok = ok && t.x != SENT && t.y != SENT;
          }
        }
        if (__all(ok) || spins > SPIN_CAP) break;
        sleep_n(4);
      }
#pragma unroll
      for (int k = 0; k < 3; ++k) {
        const int col = 128 * (G0 + (k < NG ? k : 0)) + 8 * n + 4 * (g & 1);
        const v2u t = gld8(p.x + eb * 640 + col);
        xv[k][0] = t.x; xv[k][1] = t.y;
      }
#pragma unroll
      for (int k = 0; k < 3; ++k)
#pragma unroll
        for (int i = 0; i < 4; ++i) {
          const int d = i >> 1;
          ga4[k][i] = (i & 1) ? bfhi(mv[k][0][d]) : bflo(mv[k][0][d]);
          sb4[k][i] = (i & 1) ? bfhi(mv[k][1][d]) : bflo(mv[k][1][d]);
          cb4[k][i] = 1.f + ((i & 1) ? bfhi(mv[k][2][d]) : bflo(mv[k][2][d]));
          xx4[k][i] = (i & 1) ? bfhi(xv[k][d]) : bflo(xv[k][d]);
        }
    }
    v4u sel;
#pragma unroll
    for (int t = 0; t < 4; ++t) sel[t] = (n == 2 * t ? 0x3F80u : 0u) | (n == 2 * t + 1 ? 0x3F800000u : 0u);
    v4u fg[4], fu[4], fd[8];
    if (w < 5) {
      tr_u8(ug, fg); tr_u8(uu, fu);
#pragma unroll
      for (int q = 0; q < 4; ++q) { PIN4(fg[q]); PIN4(fu[q]); }
    } else {
      tr_u16(ud, fd);
#pragma unroll
      for (int q = 0; q < 8; ++q) PIN4(fd[q]);
    }
    STAMP(11);
    int off[6]; v4u a[6];
#pragma unroll
    for (int k = 0; k < 3; ++k)
#pragma unroll
      for (int hf = 0; hf < 2; ++hf) {
        const int col = 128 * (G0 + (k < NG ? k : 0)) + 8 * n;
        off[2 * k + hf] = OFF_ATTN + ((g + 4 * hf) * 10 + (col >> 6)) * 512 + eb * 128 + (col & 63) * 2;
      }
    if (w >= 6) { const int t2 = tid - 384; trig4p(rs, OFF_CTX + (t2 & 1) * 32768 + (1 + 2 * ((t2 >> 1) & 1)) * 8192 + (t2 >> 2) * 256 + 252); }
    BAR();
    STAMP(12);
    sleep_n(AT_DELAY);
    poll16s<6>(rs, off, a, AT_SLP);
    STAMP(13);
    float x1[3][4], sm = 0.f;
#pragma unroll
    for (int k = 0; k < 3; ++k) {
      f32x4 d = mfma16(sel, a[2 * k], f32x4{0, 0, 0, 0});
      d = mfma16(sel, a[2 * k + 1], d);
#pragma unroll
      for (int i = 0; i < 4; ++i) { x1[k][i] = xx4[k][i] + ga4[k][i] * d[i]; if (k < NG) sm += x1[k][i]; }
    }
    if (!own) sm = 0.f;
    const float cnt = (float)(128 * NG);
    const float mw = wave_sum(sm) / cnt;
    float q2 = 0.f;
#pragma unroll
    for (int k = 0; k < 3; ++k)
#pragma unroll
      for (int i = 0; i < 4; ++i) { const float d = x1[k][i] - mw; if (k < NG) q2 += d * d; }
    if (!own) q2 = 0.f;
    q2 = wave_sum(q2);
    LAS float* xs = (LAS float*)(L + XE);
    if (lane == 0) { xs[2 * w] = mw; xs[2 * w + 1] = q2; }
    BAR();
    {
      const float mo = xs[2 * (w ^ 1)], qo = xs[2 * (w ^ 1) + 1];
      const float co = 640.f - cnt;
      const float m = (mw * cnt + mo * co) * (1.f / 640.f);
      const float dl = mw - mo;
      const float rstd = rsqrtf((q2 + qo + dl * dl * (cnt * co * (1.f / 640.f))) * (1.f / 640.f) + 1e-5f);
#pragma unroll
      for (int k = 0; k < 3; ++k) {
        float hv[4];
#pragma unroll
        for (int i = 0; i < 4; ++i) hv[i] = (x1[k][i] - m) * rstd * cb4[k][i] + sb4[k][i];
        if (own && k < NG) *(LAS v2u*)(L + HS + eb * 1280 + (128 * (G0 + k) + 8 * n + 4 * g) * 2) = v2u{pk2(hv[0], hv[1]), pk2(hv[2], hv[3])};
      }
    }
    BAR();
    STAMP(14);
    if (w < 5) {
      const v4u af = *(LAS v4u*)(L + HS + (n & 3) * 1280 + (128 * w + 32 * (n >> 2) + 8 * g) * 2);
      f32x4 ag[4], au[4];
#pragma unroll
      for (int q = 0; q < 4; ++q) { ag[q] = mfma16(af, fg[q], f32x4{0, 0, 0, 0}); au[q] = mfma16(af, fu[q], f32x4{0, 0, 0, 0}); }
      if ((n >> 2) == g) {
#pragma unroll
        for (int bb = 0; bb < 4; ++bb) {
          *(LAS f32x4*)(redE + (w * 4 + g) * 128 + bb * 16 + 4 * (n & 3)) = f32x4{ag[0][bb], ag[1][bb], ag[2][bb], ag[3][bb]};
          *(LAS f32x4*)(redE + (w * 4 + g) * 128 + 64 + bb * 16 + 4 * (n & 3)) = f32x4{au[0][bb], au[1][bb], au[2][bb], au[3][bb]};
        }
      }
    }
    BAR();
    STAMP(17);
    if (w >= 5) {
      f32x4 gr[5], ur[5];
#pragma unroll
      for (int k = 0; k < 5; ++k) { gr[k] = *(LAS f32x4*)(redE + (4 * k + g) * 128 + 4 * n); ur[k] = *(LAS f32x4*)(redE + (4 * k + g) * 128 + 64 + 4 * n); }
      SCHED();
      const f32x4 gg = (gr[0] + gr[1]) + (gr[2] + gr[3]) + gr[4], uu2 = (ur[0] + ur[1]) + (ur[2] + ur[3]) + ur[4];
      float gs[4], us[4];
#pragma unroll
      for (int c = 0; c < 4; ++c) { gs[c] = x32_sum(x16_sum(gg[c])); us[c] = x32_sum(x16_sum(uu2[c])); }
      const float gv = g == 0 ? gs[0] : g == 1 ? gs[1] : g == 2 ? gs[2] : gs[3];
      const float uv2 = g == 0 ? us[0] : g == 1 ? us[1] : g == 2 ? us[2] : us[3];
      const float y = 0.7978845608028654f * (gv + 0.044715f * gv * gv * gv);
      const float th = 1.f - 2.f * __builtin_amdgcn_rcpf(__expf(2.f * y) + 1.f);
      const float hd = 0.5f * gv * (1.f + th) * uv2;
      LAS unsigned char* hb = L + HID + (w - 5) * 128;
      *(LAS u16*)(hb + n * 8 + g * 2) = (u16)(pk2(hd, 0.f) & 0xFFFFu);
      LGKM0();
      v4u af = *(LAS v4u*)(hb + (n & 3) * 32 + (g & 1) * 16);
      if ((g >> 1) != ((n >> 2) & 1)) af = v4u{0, 0, 0, 0};
      f32x4 acc[8];
#pragma unroll
      for (int q = 0; q < 8; ++q) acc[q] = mfma16(af, fd[q], f32x4{0, 0, 0, 0});
      LAS unsigned char* ob = L + OST + (w - 5) * 2048;
      if (g < 2) {
#pragma unroll
        for (int bb = 0; bb < 4; ++bb) {
          v4u o;
#pragma unroll
          for (int t = 0; t < 4; ++t) o[t] = pk2(acc[2 * t][bb], acc[2 * t + 1][bb]);
          *(LAS v4u*)(ob + (8 * g + (n >> 1)) * 128 + bb * 32 + (n & 1) * 16) = san4(o);
        }
      }
      LGKM0();
#pragma unroll
      for (int i = 0; i < 2; ++i) {
        const int ln = 8 * i + (lane >> 3), grp = 16 * (w - 5) + ln;
        const v4u o = *(LAS v4u*)(ob + ln * 128 + (lane & 7) * 16);
        if (grp < 40) st16(rs, OFF_OUTP + (grp * 108 + e) * 128 + (lane & 7) * 16, o);
      }
    }
    STAMP(18);
  }
  STAMP_FLUSH();
}

__global__ void __launch_bounds__(512, 2) mk_kernel(Params p) {
  extern __shared__ __attribute__((aligned(16))) unsigned char smem[];
  LAS unsigned char* L = (LAS unsigned char*)smem;
  const int tid = threadIdx.x, lane = tid & 63;
  const int w = __builtin_amdgcn_readfirstlane(tid >> 6);
  const int x = blockIdx.x & 7, r = blockIdx.x >> 3;
  const rsrc_t rs = __builtin_amdgcn_make_buffer_rsrc((void*)p.ws, 0, WS_USED, 0x00020000);
  if (r >= 16 && w < 3) {
    unsigned long long pc;
    asm volatile("s_getpc_b64 %0" : "=s"(pc));
    const unsigned char* base = (const unsigned char*)(pc & ~255ull);
    const int o0 = ((r - 16) * 3 + w) * 1024 + lane * 16;
    if (o0 + 16 <= CODE_BYTES) { const v4u cpf0 = *(const GAS v4u*)(base + o0); asm volatile("" :: "v"(cpf0)); }
  }
  if (r < 16) ch_role(p, L, rs, x, r, tid, lane, w);
  else att_role(p, L, rs, x, r - 16, tid, lane, w);
}
}

extern "C" void kernel_launch(void* const* d_in, const int* in_sizes, int n_in, void* d_out, int out_size, void* d_ws, size_t ws_size, hipStream_t stream) {
  Params p;
  p.x = (const u16*)d_in[0]; p.ckv = (const u16*)d_in[1]; p.wq = (const u16*)d_in[2]; p.wuk = (const u16*)d_in[3];
  p.wuv = (const u16*)d_in[4]; p.wo = (const u16*)d_in[5]; p.wg = (const u16*)d_in[7]; p.wu = (const u16*)d_in[8];
  p.wd = (const u16*)d_in[9]; p.c = (const u16*)d_in[10]; p.wmod = (const u16*)d_in[11];
  p.out = (u16*)d_out; p.ws = (unsigned char*)d_ws;
  (void)hipFuncSetAttribute((const void*)mk_kernel, hipFuncAttributeMaxDynamicSharedMemorySize, LDS_BYTES);
  hipLaunchKernelGGL(mk_kernel, dim3(256), dim3(512), LDS_BYTES, stream, p);
}
```

```cpp
#include <hip/hip_runtime.h>
#include <stdint.h>

namespace {
typedef unsigned short u16;
typedef unsigned u32;
typedef u32 v4u __attribute__((ext_vector_type(4)));
typedef u32 v2u __attribute__((ext_vector_type(2)));
typedef float f32x4 __attribute__((ext_vector_type(4)));
typedef float f32x16 __attribute__((ext_vector_type(16)));
typedef __bf16 bf8 __attribute__((ext_vector_type(8)));
typedef __bf16 bf2 __attribute__((ext_vector_type(2)));
typedef short s16x4 __attribute__((ext_vector_type(4)));
#define GAS __attribute__((address_space(1)))
#define LAS __attribute__((address_space(3)))
#define DEV __device__ __forceinline__

struct Params {
  const u16 *x, *ckv, *wq, *wuk, *wuv, *wo, *wg, *wu, *wd, *c, *wmod;
  u16* out;
  unsigned char* ws;
};

constexpr int OFF_EPOCH = 0;
constexpr int OFF_FLAG1 = 1024;
constexpr int OFF_H = 4096;
constexpr int OFF_Q = 65536;
constexpr int OFF_QABS = 262144;
constexpr int OFF_STATS = 393216;
constexpr int OFF_CTX = 409600;
constexpr int OFF_ATTN = 475136;
constexpr int OFF_MODR = 516096;
constexpr int OFF_OUTP = 557056;
constexpr int OFF_O = 2 << 20;
constexpr int WS_USED = OFF_O + (4 << 20);

constexpr u32 SENT = 0xFFFFFFFFu;
constexpr int SPIN_CAP = 20000;
constexpr int AUX_SC1 = 16;
constexpr int LDS_BYTES = 163840;
#define TLB_WARM 0
#define CODE_PREFETCH 1
constexpr int CODE_BYTES = 33792;

#define ATT_DELAY 90
#define QDELAY 6
#define HSLP 2
#define CDELAY 8
#define ATT_INFL 16
#define H_TRIG 1
#define QA_DELAY 16
#define QA_SLP 2
#define AT_DELAY 16
#define AT_SLP 1
#define OU_DELAY 60
#define ATTA_DELAY 0
#define HDELAY 40
#define UK_DELAY 0
#define CH_DELAY 0

DEV float u2f(u32 u) { return __builtin_bit_cast(float, u); }
DEV u32 f2u(float f) { return __builtin_bit_cast(u32, f); }
DEV float bflo(u32 d) { return u2f(d << 16); }
DEV float bfhi(u32 d) { return u2f(d & 0xFFFF0000u); }
DEV u32 pk2(float lo, float hi) { bf2 v = {(__bf16)lo, (__bf16)hi}; return __builtin_bit_cast(u32, v); }
DEV u32 san(u32 d) { return d < 0xFFFFFFFEu ? d : 0xFFFFFFFEu; }
DEV v4u san4(v4u v) { v.x = san(v.x); v.y = san(v.y); v.z = san(v.z); v.w = san(v.w); return v; }
DEV bool ok4(v4u v) { u32 a = v.x > v.y ? v.x : v.y, b = v.z > v.w ? v.z : v.w; return (a > b ? a : b) != SENT; }
DEV f32x4 mfma16(v4u a, v4u b, f32x4 c) { return __builtin_amdgcn_mfma_f32_16x16x32_bf16(__builtin_bit_cast(bf8, a), __builtin_bit_cast(bf8, b), c, 0, 0, 0); }
DEV f32x16 mfma32(v4u a, v4u b, f32x16 c) { return __builtin_amdgcn_mfma_f32_32x32x16_bf16(__builtin_bit_cast(bf8, a), __builtin_bit_cast(bf8, b), c, 0, 0, 0); }
template <int C> DEV float dppf(float v) { return u2f((u32)__builtin_amdgcn_update_dpp(0, (int)f2u(v), C, 0xf, 0xf, false)); }
DEV float row_sum(float v) { v += dppf<0xB1>(v); v += dppf<0x4E>(v); v += dppf<0x141>(v); v += dppf<0x140>(v); return v; }
DEV float row_max(float v) { v = fmaxf(v, dppf<0xB1>(v)); v = fmaxf(v, dppf<0x4E>(v)); v = fmaxf(v, dppf<0x141>(v)); v = fmaxf(v, dppf<0x140>(v)); return v; }
DEV float x16_sum(float v) { auto r = __builtin_amdgcn_permlane16_swap(f2u(v), f2u(v), false, false); return u2f(r[0]) + u2f(r[1]); }
DEV float x32_sum(float v) { auto r = __builtin_amdgcn_permlane32_swap(f2u(v), f2u(v), false, false); return u2f(r[0]) + u2f(r[1]); }
DEV float x16_max(float v) { auto r = __builtin_amdgcn_permlane16_swap(f2u(v), f2u(v), false, false); return fmaxf(u2f(r[0]), u2f(r[1])); }
DEV float x32_max(float v) { auto r = __builtin_amdgcn_permlane32_swap(f2u(v), f2u(v), false, false); return fmaxf(u2f(r[0]), u2f(r[1])); }
DEV float wave_sum(float v) { return x32_sum(x16_sum(row_sum(v))); }
DEV float silu(float v) { return v / (1.f + __expf(-v)); }

#define BAR() do { asm volatile("s_waitcnt lgkmcnt(0)" ::: "memory"); __builtin_amdgcn_s_barrier(); asm volatile("" ::: "memory"); } while (0)
#define DRAIN() asm volatile("s_waitcnt vmcnt(0)" ::: "memory")
#define LGKM0() asm volatile("s_waitcnt lgkmcnt(0)" ::: "memory")
#define SCHED() __builtin_amdgcn_sched_barrier(0)
#define PIN4(v) asm volatile("" : "+v"(v))

typedef __amdgpu_buffer_rsrc_t rsrc_t;
DEV v4u ld16(rsrc_t rs, int off) { return __builtin_amdgcn_raw_buffer_load_b128(rs, off, 0, AUX_SC1); }
DEV u32 ld4(rsrc_t rs, int off) { return __builtin_amdgcn_raw_buffer_load_b32(rs, off, 0, AUX_SC1); }
DEV void st16(rsrc_t rs, int off, v4u v) { __builtin_amdgcn_raw_buffer_store_b128(v, rs, off, 0, AUX_SC1); }
DEV void st4(rsrc_t rs, int off, u32 v) { __builtin_amdgcn_raw_buffer_store_b32(v, rs, off, 0, AUX_SC1); }
DEV v4u gld16(const u16* p) { return *(const GAS v4u*)p; }
DEV v2u gld8(const u16* p) { return *(const GAS v2u*)p; }
DEV v4u gld16nt(const u16* p) { return __builtin_nontemporal_load((const GAS v4u*)p); }
DEV v2u gld8nt(const u16* p) { return __builtin_nontemporal_load((const GAS v2u*)p); }

DEV void dma16_nt(const void* src, int dst) {
  u32 keep;
  asm volatile("s_mov_b32 %0, m0\n\ts_mov_b32 m0, %2\n\ts_nop 0\n\tglobal_load_lds_dwordx4 %1, off nt\n\ts_mov_b32 m0, %0" : "=&s"(keep) : "v"(src), "s"(dst) : "memory");
}
template <int NP, int RS> DEV f32x4 reduce256(LAS float* red, int w, int lane) {
  const int G = (w >> 1) * 16 + (lane & 1) * 8 + 4 * (w & 1) + ((lane >> 1) & 3), pg = lane >> 3;
  constexpr int NK = (NP + 7) / 8;
  f32x4 v[NK];
#pragma unroll
  for (int k = 0; k < NK; ++k) { const bool ok = 8 * k + 8 <= NP || pg + 8 * k < NP; v[k] = *(LAS f32x4*)(red + (ok ? pg + 8 * k : pg) * RS + 4 * G); }
  SCHED();
  f32x4 s = v[0];
#pragma unroll
  for (int k = 1; k < NK; ++k) { const bool ok = 8 * k + 8 <= NP || pg + 8 * k < NP; s += ok ? v[k] : f32x4{0, 0, 0, 0}; }
#pragma unroll
  for (int c = 0; c < 4; ++c) { float t = s[c]; t += dppf<0x128>(t); t = x32_sum(x16_sum(t)); s[c] = t; }
  return s;
}
template <int NP, int RS> DEV f32x4 reduce256w(LAS float* red, int lane) {
  f32x4 v[NP];
#pragma unroll
  for (int k = 0; k < NP; ++k) v[k] = *(LAS f32x4*)(red + k * RS + 4 * lane);
  SCHED();
#pragma unroll
  for (int st = 1; st < NP; st *= 2)
#pragma unroll
    for (int k = 0; k + st < NP; k += 2 * st) v[k] += v[k + st];
  return v[0];
}
template <int NP, int RS> DEV f32x4 reduce256q(LAS float* red, int w, int lane) {
  const int G = 16 * w + (lane & 15), pg = lane >> 4;
  constexpr int NK = (NP + 3) / 4;
  f32x4 v[NK];
#pragma unroll
  for (int k = 0; k < NK; ++k) { const bool ok = 4 * k + 4 <= NP || pg + 4 * k < NP; v[k] = *(LAS f32x4*)(red + (ok ? pg + 4 * k : pg) * RS + 4 * G); }
  SCHED();
  f32x4 s = v[0];
#pragma unroll
  for (int k = 1; k < NK; ++k) { const bool ok = 4 * k + 4 <= NP || pg + 4 * k < NP; s += ok ? v[k] : f32x4{0, 0, 0, 0}; }
#pragma unroll
  for (int c = 0; c < 4; ++c) s[c] = x32_sum(x16_sum(s[c]));
  return s;
}
DEV v4u pack8_pairs(f32x4 s) {
  const u32 a = pk2(s[0], s[1]), b = pk2(s[2], s[3]);
  const u32 na = (u32)__builtin_amdgcn_update_dpp(0, (int)a, 0xB1, 0xf, 0xf, false), nb = (u32)__builtin_amdgcn_update_dpp(0, (int)b, 0xB1, 0xf, 0xf, false);
  return v4u{a, b, na, nb};
}
DEV v4u pack8_ror(f32x4 s) {
  const u32 a = pk2(s[0], s[1]), b = pk2(s[2], s[3]);
  const u32 na = (u32)__builtin_amdgcn_update_dpp(0, (int)a, 0x128, 0xf, 0xf, false), nb = (u32)__builtin_amdgcn_update_dpp(0, (int)b, 0x128, 0xf, 0xf, false);
  return v4u{a, b, na, nb};
}
DEV void sleep_n(int n) { for (int i = 0; i < n; ++i) __builtin_amdgcn_s_sleep(1); }

template <int N> DEV void poll16(rsrc_t rs, const int (&off)[N], v4u (&v)[N]) {
  for (int spins = 0;; ++spins) {
    asm volatile("" ::: "memory");
#pragma unroll
    for (int i = 0; i < N; ++i) v[i] = ld16(rs, off[i]);
    bool ok = true;
#pragma unroll
    for (int i = 0; i < N; ++i) ok = ok && ok4(v[i]);
    if (__all(ok) || spins > SPIN_CAP) break;
  }
}
template <int N> DEV void poll16s(rsrc_t rs, const int (&off)[N], v4u (&v)[N], int slp) {
  for (int spins = 0;; ++spins) {
    asm volatile("" ::: "memory");
#pragma unroll
    for (int i = 0; i < N; ++i) v[i] = ld16(rs, off[i]);
    bool ok = true;
#pragma unroll
    for (int i = 0; i < N; ++i) ok = ok && ok4(v[i]);
    if (__all(ok) || spins > SPIN_CAP) break;
    sleep_n(slp);
  }
}
template <int N> DEV void pollg(rsrc_t rs, const int (&off)[N], v4u (&v)[N], u32 tag) {
  for (int spins = 0;; ++spins) {
    asm volatile("" ::: "memory");
#pragma unroll
    for (int i = 0; i < N; ++i) v[i] = ld16(rs, off[i]);
    bool ok = true;
#pragma unroll
    for (int i = 0; i < N; ++i) ok = ok && v[i].y == tag && v[i].w == tag;
    if (__all(ok) || spins > SPIN_CAP) break;
  }
}
DEV void trig4(rsrc_t rs, int off) {
  for (int spins = 0;; ++spins) {
    asm volatile("" ::: "memory");
    const u32 v = ld4(rs, off);
    if (__all(v != SENT) || spins > SPIN_CAP) break;
    __builtin_amdgcn_s_sleep(1);
  }
}
DEV void trig4p(rsrc_t rs, int off) {
  asm volatile("" ::: "memory");
  u32 v0 = ld4(rs, off);
  __builtin_amdgcn_s_sleep(2);
  asm volatile("" ::: "memory");
  u32 v1 = ld4(rs, off);
  __builtin_amdgcn_s_sleep(2);
  asm volatile("" ::: "memory");
  u32 v2 = ld4(rs, off);
  for (int spins = 0; spins < SPIN_CAP; ++spins) {
    if (__all(v0 != SENT)) break;
    asm volatile("" ::: "memory");
    v0 = ld4(rs, off);
    if (__all(v1 != SENT)) break;
    asm volatile("" ::: "memory");
    v1 = ld4(rs, off);
    if (__all(v2 != SENT)) break;
    asm volatile("" ::: "memory");
    v2 = ld4(rs, off);
  }
}
DEV void trigtp(rsrc_t rs, int off, u32 tag) {
  asm volatile("" ::: "memory");
  u32 v0 = ld4(rs, off);
  __builtin_amdgcn_s_sleep(2);
  asm volatile("" ::: "memory");
  u32 v1 = ld4(rs, off);
  __builtin_amdgcn_s_sleep(2);
  asm volatile("" ::: "memory");
  u32 v2 = ld4(rs, off);
  for (int spins = 0; spins < SPIN_CAP; ++spins) {
    if (__all(v0 == tag)) break;
    asm volatile("" ::: "memory");
    v0 = ld4(rs, off);
    if (__all(v1 == tag)) break;
    asm volatile("" ::: "memory");
    v1 = ld4(rs, off);
    if (__all(v2 == tag)) break;
    asm volatile("" ::: "memory");
    v2 = ld4(rs, off);
  }
}
DEV void trigt(rsrc_t rs, int off, u32 tag) {
  for (int spins = 0;; ++spins) {
    asm volatile("" ::: "memory");
    const u32 v = ld4(rs, off);
    if (__all(v == tag) || spins > SPIN_CAP) break;
    __builtin_amdgcn_s_sleep(1);
  }
}
template <int N> DEV void pollgs(rsrc_t rs, const int (&off)[N], v4u (&v)[N], u32 tag, int slp) {
  for (int spins = 0;; ++spins) {
    asm volatile("" ::: "memory");
#pragma unroll
    for (int i = 0; i < N; ++i) v[i] = ld16(rs, off[i]);
    bool ok = true;
#pragma unroll
    for (int i = 0; i < N; ++i) ok = ok && v[i].y == tag && v[i].w == tag;
    if (__all(ok) || spins > SPIN_CAP) break;
    sleep_n(slp);
  }
}
DEV void poll_flags128(rsrc_t rs, int base, int lane, u32 tag) {
  for (int spins = 0;; ++spins) {
    asm volatile("" ::: "memory");
    const v4u f = ld16(rs, base + (lane & 31) * 16);
    if (__all(f.x == tag && f.y == tag && f.z == tag && f.w == tag) || spins > SPIN_CAP) break;
  }
}

struct U16 { v4u w[8]; };
struct U8 { v2u w[8]; };
template <bool NT> DEV void load_u16(U16& U, const u16* base, int ld, int lane) {
  const int g = lane >> 4, n = lane & 15;
  const u16* p = base + (size_t)(32 * (n >> 2) + 8 * g) * ld + 8 * (n & 3);
#pragma unroll
  for (int i = 0; i < 8; ++i) U.w[i] = NT ? gld16nt(p + (size_t)i * ld) : gld16(p + (size_t)i * ld);
}
template <bool NT> DEV void load_u16w(U16& U, const u16* base, int ld, int lane) {
  const int g = lane >> 4, n = lane & 15;
  const u16* p = base + (size_t)(32 * (n >> 3) + 8 * g) * ld + 8 * (n & 7);
#pragma unroll
  for (int i = 0; i < 8; ++i) U.w[i] = NT ? gld16nt(p + (size_t)i * ld) : gld16(p + (size_t)i * ld);
}
template <bool NT> DEV void load_u8(U8& U, const u16* base, int ld, int lane) {
  const int g = lane >> 4, n = lane & 15;
  const u16* p = base + (size_t)(32 * (n >> 2) + 8 * g) * ld + 4 * (n & 3);
#pragma unroll
  for (int i = 0; i < 8; ++i) U.w[i] = NT ? gld8nt(p + (size_t)i * ld) : gld8(p + (size_t)i * ld);
}
DEV void tr_u16(const U16& U, v4u (&f)[8]) {
#pragma unroll
  for (int j = 0; j < 8; ++j) {
    const u32 sel = (j & 1) ? 0x07060302u : 0x05040100u;
#pragma unroll
    for (int t = 0; t < 4; ++t) f[j][t] = __builtin_amdgcn_perm(U.w[2 * t + 1][j >> 1], U.w[2 * t][j >> 1], sel);
  }
}
DEV void tr_u8(const U8& U, v4u (&f)[4]) {
#pragma unroll
  for (int j = 0; j < 4; ++j) {
    const u32 sel = (j & 1) ? 0x07060302u : 0x05040100u;
#pragma unroll
    for (int t = 0; t < 4; ++t) f[j][t] = __builtin_amdgcn_perm(U.w[2 * t + 1][j >> 1], U.w[2 * t][j >> 1], sel);
  }
}
DEV v4u silu_frag(v4u cv) {
  v4u o;
#pragma unroll
  for (int t = 0; t < 4; ++t) o[t] = pk2(silu(bflo(cv[t])), silu(bfhi(cv[t])));
  return o;
}

template <bool PARTS> DEV void put_partials(LAS float* row0, int RSf, const f32x4 (&acc)[8], int w, int g, int n) {
  const bool hi = n >> 3;
  float v[8];
#pragma unroll
  for (int q = 0; q < 8; ++q) v[q] = PARTS ? (hi ? acc[q][1] + acc[q][3] : acc[q][0] + acc[q][2]) : (hi ? acc[q][1] : acc[q][0]);
  LAS float* dst = row0 + (w * 2 + (n >> 3)) * RSf + g * 64 + 4 * (n & 7);
  *(LAS f32x4*)dst = f32x4{v[0], v[1], v[2], v[3]};
  *(LAS f32x4*)(dst + 32) = f32x4{v[4], v[5], v[6], v[7]};
}
DEV int fsw(int row) { return ((row & 3) << 2) | ((row >> 2) & 3); }
constexpr int RS = 288;

DEV void ch_role(const Params& p, LAS unsigned char* L, rsrc_t rs, int x, int r, int tid, int lane, int w) {

  const int h = 4 * x + (r >> 2), j = r & 3, dh = j & 1, kh = j >> 1;
  const int chi = 16 * x + r;
  const int g = lane >> 4, n = lane & 15;
  const int bp = j & 1, chf = j >> 1;
  const bool isD3 = r < 10;
  const int id3 = 10 * x + r, ks3 = id3 / 10, slab3 = id3 % 10;
  const bool isM = r >= 10 && r < 15;
  const int slabM = 5 * x + (r - 10);
  constexpr int UKS = 0;
  constexpr int RED_A = 32768, RED_Q = RED_A + 10 * RS * 4, RED_D = RED_Q + 20 * RS * 4, RED_3 = RED_D + 32 * RS * 4, STG = RED_3 + 32 * RS * 4, XST = STG + 2048, QST = XST + 128, RED_F = QST + 2048;
  static_assert(RED_F + 2048 <= LDS_BYTES, "lds");
  LAS float* redA = (LAS float*)(L + RED_A);
  LAS float* redQ = (LAS float*)(L + RED_Q);
  LAS float* redD = (LAS float*)(L + RED_D);
  LAS float* red3 = (LAS float*)(L + RED_3);
  LAS float* xst = (LAS float*)(L + XST);
  const v4u S4 = {SENT, SENT, SENT, SENT};
  const GAS u32* epp = (const GAS u32*)(p.ws + OFF_EPOCH);

  auto uk_dma = [&](int i0, int i1) {
#pragma unroll
    for (int i = i0; i < i1; ++i) {
      const int row = 4 * i + (lane >> 4);
      const int ch = (lane & 15) ^ fsw(row);
      dma16_nt(p.wuk + ((size_t)(128 * j + row) * 32 + h) * 128 + ch * 8, UKS + 4 * i * 256);
    }
    SCHED();
  };

  U16 uq;
  u32 ep;
  v4u fq[8];
  if (w < 5) {
    sleep_n(CH_DELAY);
    load_u16w<true>(uq, p.wq + (size_t)(320 * kh + 64 * w) * 4096 + h * 128 + 64 * dh, 4096, lane);
    ep = __hip_atomic_load(epp, __ATOMIC_RELAXED, __HIP_MEMORY_SCOPE_AGENT);
    SCHED();
  } else if (w == 7) {
    ep = __hip_atomic_load(epp, __ATOMIC_RELAXED, __HIP_MEMORY_SCOPE_AGENT);
    asm volatile("" ::: "memory");
    st16(rs, OFF_QABS + (lane >> 4) * 32768 + h * 1024 + j * 256 + (lane & 15) * 16, S4);
    if (lane < 32) st16(rs, OFF_CTX + chf * 32768 + (2 * bp + (lane >> 4)) * 8192 + h * 256 + (lane & 15) * 16, S4);
    else if (lane < 40) st16(rs, OFF_STATS + chi * 128 + (lane - 32) * 16, S4);
    if (lane < 32) { if (chi < 80) st16(rs, OFF_ATTN + chi * 512 + lane * 16, S4); }
    else { if (chi < 40) st16(rs, OFF_MODR + chi * 512 + (lane - 32) * 16, S4); }
    if (chi < 108) {
#pragma unroll
      for (int i = 0; i < 5; ++i) st16(rs, OFF_OUTP + ((8 * i + (lane >> 3)) * 108 + chi) * 128 + (lane & 7) * 16, S4);
    }

    DRAIN();
    if (lane == 0) st4(rs, OFF_FLAG1 + chi * 4, ep + 1u);

  } else if (w == 6) {
    ep = 0;
    sleep_n(UK_DELAY);
    uk_dma(0, 32);
  } else {
    ep = __hip_atomic_load(epp, __ATOMIC_RELAXED, __HIP_MEMORY_SCOPE_AGENT);
  }
  const u32 tag = ep + 1u;
  if (w < 5) {
    tr_u16(uq, fq);
#pragma unroll
    for (int q = 0; q < 8; ++q) PIN4(fq[q]);

    const int o = OFF_H + (((n >> 1) & 1) * 10 + 5 * kh + w) * 1024 + (n >> 2) * 256 + 128 * (n & 1) + 32 * g;
    int off[4] = {o, o + 16, o + 20480, o + 20480 + 16};
    v4u a[4];
    BAR();

    pollgs<4>(rs, off, a, tag, HSLP);

    const v4u af0 = {a[0].x, a[0].z, a[1].x, a[1].z}, af1 = {a[2].x, a[2].z, a[3].x, a[3].z};
    f32x4 acc[8];
#pragma unroll
    for (int q = 0; q < 8; ++q) { acc[q] = mfma16(af0, fq[q], f32x4{0, 0, 0, 0}); acc[q] = mfma16(af1, fq[q], acc[q]); }
    put_partials<true>(redQ, RS, acc, w, g, n);
  }
  if (w >= 5) {
    if (w == 7) { const int l20 = lane < 20 ? lane : 0; trigtp(rs, OFF_H + ((l20 / 5) * 10 + 5 * kh + (l20 % 5)) * 1024 + 1020, tag); }
    BAR();
  }
  BAR();

  if (w < 6 || w == 7) {
  }
  {
    if (w == 6) ep = __hip_atomic_load(epp, __ATOMIC_RELAXED, __HIP_MEMORY_SCOPE_AGENT);
    const u32 tg = ep + 1u;
    const f32x4 v = reduce256<10, RS>(redQ, w, lane);
    if (lane < 8) st16(rs, OFF_Q + (h * 4 + j) * 1024 + (8 * w + lane) * 16, v4u{pk2(v[0], v[1]), tg, pk2(v[2], v[3]), tg});
  }

  {
    if (w < 4) {
      int off[1] = {OFF_Q + h * 4096 + tid * 16}; v4u a[1];
      sleep_n(QDELAY);
      pollg<1>(rs, off, a, tag);
      *(LAS v2u*)(L + QST + tid * 8) = v2u{a[0].x, a[0].z};
    } else if (w == 6) DRAIN();
    BAR();

    f32x4 acc = {0, 0, 0, 0};
    const int row = 16 * w + n;
    v4u af[4], bfr[4];
#pragma unroll
    for (int t = 0; t < 4; ++t) {
      af[t] = *(LAS v4u*)(L + QST + ((((n >> 2) & 1) << 1) + (t >> 1)) * 512 + (n & 3) * 128 + (32 * (t & 1) + 8 * g) * 2);
      bfr[t] = *(LAS v4u*)(L + UKS + row * 256 + (((4 * t + g) ^ fsw(row)) << 4));
    }
#pragma unroll
    for (int t = 0; t < 4; ++t) acc = mfma16(af[t], bfr[t], acc);
    float v;
    {
      const float s0 = x16_sum(acc[0]), s1 = x16_sum(acc[1]), s2 = x16_sum(acc[2]), s3 = x16_sum(acc[3]);
      v = (g == 0 ? s0 : g == 1 ? s1 : g == 2 ? s2 : s3) * (0.08838834764831845f * 1.4426950408889634f);
    }
    LAS u16* stg = (LAS u16*)(L + STG);
    stg[g * 128 + 16 * w + n] = (u16)(pk2(v, 0.f) & 0xFFFFu);
  }
  BAR();
  if (w == 0) {
    const v4u v = *(LAS v4u*)(L + STG + lane * 16);
    st16(rs, OFF_QABS + (lane >> 4) * 32768 + h * 1024 + j * 256 + (lane & 15) * 16, san4(v));
  }
  BAR();

  U16 uv, uo, um[2]; v4u cf[2];
  {
    const u16* pv = p.wuv + ((size_t)(256 * chf + 32 * w + 8 * g) * 32 + h) * 128 + 8 * n;
#pragma unroll
    for (int i = 0; i < 8; ++i) uv.w[i] = gld16nt(pv + (size_t)i * 4096);
    if (isD3) load_u16w<true>(uo, p.wo + (size_t)(512 * ks3 + 64 * w) * 640 + 64 * slab3, 640, lane);
    if (isM && w < 5) {
#pragma unroll
      for (int u = 0; u < 2; ++u) {
        cf[u] = gld16(p.c + (n >> 2) * 640 + 128 * w + 64 * u + 32 * (n & 1) + 8 * g);
        load_u16w<true>(um[u], p.wmod + (size_t)(128 * w + 64 * u) * 3840 + 1280 + 64 * slabM, 3840, lane);
      }
    }
    SCHED();
  }

  if (w == 7) poll_flags128(rs, OFF_FLAG1, lane, tag);
  v4u fv[8], fo[8];
  tr_u16(uv, fv);
#pragma unroll
  for (int q = 0; q < 8; ++q) PIN4(fv[q]);
  if (isD3) {
    tr_u16(uo, fo);
#pragma unroll
    for (int q = 0; q < 8; ++q) PIN4(fo[q]);
  }
  if (isM && w < 5) {
    f32x4 acc[8];
#pragma unroll
    for (int q = 0; q < 8; ++q) acc[q] = f32x4{0, 0, 0, 0};
#pragma unroll
    for (int u = 0; u < 2; ++u) {
      v4u fm[8];
      tr_u16(um[u], fm);
      const v4u af = silu_frag(cf[u]);
#pragma unroll
      for (int q = 0; q < 8; ++q) acc[q] = mfma16(af, fm[q], acc[q]);
    }
    put_partials<false>(red3, RS, acc, w, g, n);
  }
  BAR();
  if (isM && w < 4) {
    const f32x4 v = reduce256q<10, RS>(red3, w, lane);
    const v4u o = pack8_ror(v);
    if (lane < 8) st16(rs, OFF_MODR + slabM * 512 + w * 128 + lane * 16, san4(o));
  }

  {
    int off[4]; v4u a[4];
#pragma unroll
    for (int mt = 0; mt < 4; ++mt) off[mt] = OFF_O + (((2 * bp + (mt >> 1)) * 32 + h) * 2 + chf) * 16384 + (16 * (mt & 1) + n) * 512 + 64 * w + 16 * g;
    const int so = OFF_STATS + ((2 * bp + (lane >> 5)) * 32 + (lane & 31)) * 128 + h * 4;
    if (w == 7) trig4p(rs, so);
    BAR();

    const u32 lse_u = ld4(rs, so);
#pragma unroll
    for (int mt = 0; mt < 4; ++mt) a[mt] = ld16(rs, off[mt]);
    SCHED();
    const float lse = u2f(lse_u);
    const float mx = x16_max(row_max(lse));
    const float ex = __builtin_amdgcn_exp2f(lse - mx);
    const float al = ex / x16_sum(row_sum(ex));
    float wt[2][8];
#pragma unroll
    for (int bb = 0; bb < 2; ++bb)
#pragma unroll
      for (int mt = 0; mt < 2; ++mt)
#pragma unroll
        for (int i = 0; i < 4; ++i) wt[bb][mt * 4 + i] = __shfl(al, 32 * bb + 16 * mt + 4 * g + i);
#pragma unroll
    for (int bb = 0; bb < 2; ++bb) {
      f32x4 acc[2][8];
#pragma unroll
      for (int mt = 0; mt < 2; ++mt)
#pragma unroll
        for (int q = 0; q < 8; ++q) acc[mt][q] = mfma16(a[2 * bb + mt], fv[q], f32x4{0, 0, 0, 0});

      const f32x4 w0 = {wt[bb][0], wt[bb][1], wt[bb][2], wt[bb][3]}, w1 = {wt[bb][4], wt[bb][5], wt[bb][6], wt[bb][7]};
      float res[8];
#pragma unroll
      for (int q = 0; q < 8; ++q) { const f32x4 t = acc[0][q] * w0 + acc[1][q] * w1; res[q] = (t[0] + t[1]) + (t[2] + t[3]); }
      *(LAS f32x4*)(redD + (w * 4 + g) * RS + bb * 128 + 4 * n) = f32x4{res[0], res[1], res[2], res[3]};
      *(LAS f32x4*)(redD + (w * 4 + g) * RS + bb * 128 + 64 + 4 * n) = f32x4{res[4], res[5], res[6], res[7]};
    }
  }

  BAR();
  if (w < 4) {
    const int pos = (w >> 1) * 128 + ((lane >> 3) & 1) * 64 + 4 * (8 * (w & 1) + (lane & 7)), pg = lane >> 4;
    f32x4 vv[8];
#pragma unroll
    for (int k = 0; k < 8; ++k) vv[k] = *(LAS f32x4*)(redD + (pg + 4 * k) * RS + pos);
    SCHED();
    f32x4 v = ((vv[0] + vv[1]) + (vv[2] + vv[3])) + ((vv[4] + vv[5]) + (vv[6] + vv[7]));
#pragma unroll
    for (int c = 0; c < 4; ++c) v[c] = x32_sum(x16_sum(v[c]));
    const v4u o = pack8_ror(v);
    if (lane < 8) st16(rs, OFF_CTX + chf * 32768 + (2 * bp + (w >> 1)) * 8192 + h * 256 + (w & 1) * 128 + lane * 16, san4(o));
  }

  if (isD3) {
    const int k0 = 512 * ks3 + 64 * w + 32 * (n & 1) + 8 * g;
    int off[1] = {OFF_CTX + ((n >> 1) & 1) * 32768 + (n >> 2) * 8192 + k0 * 2};
    v4u a[1];
    sleep_n(CDELAY);
    poll16s<1>(rs, off, a, 1);

    f32x4 acc[8];
#pragma unroll
    for (int q = 0; q < 8; ++q) acc[q] = mfma16(a[0], fo[q], f32x4{0, 0, 0, 0});
    put_partials<true>(red3, RS, acc, w, g, n);
    BAR();
    if (w < 4) {
      const f32x4 v = reduce256q<16, RS>(red3, w, lane);
      const v4u o = pack8_ror(v);
      if (lane < 8) st16(rs, OFF_ATTN + id3 * 512 + w * 128 + lane * 16, san4(o));
    }

  }
  if (isM) {
    float x1 = 0.f, gbv = 0.f;
    if (w == 0) {
      const int bb = lane >> 4, col = 16 * slabM + (lane & 15);
      const int sub = bb * 128 + ((col & 63) * 2 & ~3);
      const u32 xv = (u32)p.x[bb * 640 + col];
      u32 v[10];
      for (int spins = 0;; ++spins) {
        asm volatile("" ::: "memory");
#pragma unroll
        for (int k = 0; k < 8; ++k) v[k] = ld4(rs, OFF_ATTN + (k * 10 + (col >> 6)) * 512 + sub);
        v[8] = ld4(rs, OFF_MODR + (0 * 10 + (col >> 6)) * 512 + sub);
        v[9] = ld4(rs, OFF_MODR + (3 * 10 + (col >> 6)) * 512 + sub);
        bool ok = true;
#pragma unroll
        for (int k = 0; k < 10; ++k) ok = ok && v[k] != SENT;
        if (__all(ok) || spins > SPIN_CAP) break;
        __builtin_amdgcn_s_sleep(4);
      }
      if (slabM == 0 && lane == 0) st4(rs, OFF_EPOCH, tag);
      float f[10], at = 0.f;
#pragma unroll
      for (int k = 0; k < 10; ++k) f[k] = (col & 1) ? bfhi(v[k]) : bflo(v[k]);
#pragma unroll
      for (int k = 0; k < 8; ++k) at += f[k];
      x1 = bflo(xv) + f[8] * at;
      gbv = f[9];
    }
    BAR();

    const int e0 = tid >> 3, e1 = (64 + e0 < 108) ? 64 + e0 : e0;
    int off[2] = {OFF_OUTP + (slabM * 108 + e0) * 128 + (tid & 7) * 16, OFF_OUTP + (slabM * 108 + e1) * 128 + (tid & 7) * 16};
    v4u a[2];
    sleep_n(OU_DELAY);
    poll16s<2>(rs, off, a, 1);

    float v8[8];
    const float m1 = (64 + e0 < 108) ? 1.f : 0.f;
#pragma unroll
    for (int t = 0; t < 4; ++t) { v8[2 * t] = bflo(a[0][t]) + m1 * bflo(a[1][t]); v8[2 * t + 1] = bfhi(a[0][t]) + m1 * bfhi(a[1][t]); }
#pragma unroll
    for (int t = 0; t < 8; ++t) { float v = v8[t]; v += dppf<0x128>(v); v = x32_sum(x16_sum(v)); v8[t] = v; }
    LAS float* redf = (LAS float*)(L + RED_F);
    if (lane < 8) {
      *(LAS f32x4*)(redf + w * 64 + lane * 8) = f32x4{v8[0], v8[1], v8[2], v8[3]};
      *(LAS f32x4*)(redf + w * 64 + lane * 8 + 4) = f32x4{v8[4], v8[5], v8[6], v8[7]};
    }
    BAR();
    if (w == 0) {
      float y = 0.f;
#pragma unroll
      for (int i = 0; i < 8; ++i) y += redf[i * 64 + lane];
      const float o = x1 + gbv * y;
      p.out[(lane >> 4) * 640 + 16 * slabM + (lane & 15)] = (u16)(pk2(o, 0.f) & 0xFFFFu);
    }

  }

}

DEV void att_role(const Params& p, LAS unsigned char* L, rsrc_t rs, int x, int r, int tid, int lane, int w) {

  const int b = x >> 1, s = ((x & 1) << 4) | r;
  const int e = 14 * x + r;
  const bool isE = r < 14 && e < 108;
  const int g = lane >> 4, n = lane & 15, hh = lane >> 5, m32 = lane & 31;
  constexpr int QS = 131072;
  const bool isA = r < 5;
  if (isA) {
    const int ia = 5 * x + r, slA = ia % 10, matA = (ia / 10) & 1, khA = ia / 20;
    LAS float* redA = (LAS float*)(L + QS);
    LAS float* xst = (LAS float*)(L + QS + 10 * RS * 4);
    if (w < 5) {
      U16 ua;
      const v4u cfr = gld16(p.c + (n >> 2) * 640 + 320 * khA + 64 * w + 32 * (n & 1) + 8 * g);
      load_u16w<true>(ua, p.wmod + (size_t)(320 * khA + 64 * w) * 3840 + 640 * matA + 64 * slA, 3840, lane);
      SCHED();
      const v4u af = silu_frag(cfr);
      v4u fa[8];
      tr_u16(ua, fa);

      f32x4 acc[8];
#pragma unroll
      for (int q = 0; q < 8; ++q) acc[q] = mfma16(af, fa[q], f32x4{0, 0, 0, 0});
      put_partials<false>(redA, RS, acc, w, g, n);
      BAR();
    } else if (w == 6) {
      u32 d[4][5];
#pragma unroll
      for (int bb = 0; bb < 4; ++bb)
#pragma unroll
        for (int i = 0; i < 5; ++i) d[bb][i] = *(const GAS u32*)(p.x + bb * 640 + 2 * (lane + 64 * i));
      SCHED();
#pragma unroll
      for (int bb = 0; bb < 4; ++bb) {
        float sx = 0.f;
#pragma unroll
        for (int i = 0; i < 5; ++i) sx += bflo(d[bb][i]) + bfhi(d[bb][i]);
        const float m = wave_sum(sx) * (1.f / 640.f);
        float q2 = 0.f;
#pragma unroll
        for (int i = 0; i < 5; ++i) { const float d0 = bflo(d[bb][i]) - m, d1 = bfhi(d[bb][i]) - m; q2 += d0 * d0 + d1 * d1; }
        const float var = wave_sum(q2) * (1.f / 640.f);
        if (lane == 0) { xst[2 * bb] = m; xst[2 * bb + 1] = rsqrtf(var + 1e-5f); }
      }
      BAR();
    } else if (w == 5) {
      const u32 tg = __hip_atomic_load((GAS u32*)(p.ws + OFF_EPOCH), __ATOMIC_RELAXED, __HIP_MEMORY_SCOPE_AGENT) + 1u;
      const v2u xa2 = gld8(p.x + (lane >> 4) * 640 + 64 * slA + 8 * (lane & 7) + 4 * ((lane >> 3) & 1));
      BAR();
      f32x4 v = reduce256w<10, RS>(redA, lane);
      if (matA) {
        const float m = xst[2 * (lane >> 4)], rstd = xst[2 * (lane >> 4) + 1], one = khA == 0 ? 1.f : 0.f;
        v[0] = (bflo(xa2.x) - m) * rstd * (v[0] + one); v[1] = (bfhi(xa2.x) - m) * rstd * (v[1] + one);
        v[2] = (bflo(xa2.y) - m) * rstd * (v[2] + one); v[3] = (bfhi(xa2.y) - m) * rstd * (v[3] + one);
      }
      st16(rs, OFF_H + ((matA + 2 * khA) * 10 + slA) * 1024 + (lane >> 4) * 256 + (lane & 7) * 32 + ((lane >> 3) & 1) * 16, v4u{pk2(v[0], v[1]), tg, pk2(v[2], v[3]), tg});

    } else BAR();
    BAR();
    sleep_n(ATTA_DELAY);
  } else sleep_n(ATT_DELAY);
  {
    const u16* cb = p.ckv + ((size_t)b * 4096 + s * 128 + 16 * w) * 512;
#pragma unroll
    for (int i = 0; i < 16; ++i) {
      const int row = 16 * w + i;
      const int ch = (lane & ~15) | ((lane & 15) ^ fsw(row));
      dma16_nt(cb + (size_t)i * 512 + ch * 8, row * 1024);
    }
  }
  const u32 tag = __hip_atomic_load((GAS u32*)(p.ws + OFF_EPOCH), __ATOMIC_RELAXED, __HIP_MEMORY_SCOPE_AGENT) + 1u;

  if (w == 7) poll_flags128(rs, OFF_FLAG1, lane, tag);
  DRAIN();
  BAR();

  v4u ka[32];
  if (w < 4) {
    const int row = 32 * w + m32;
    const int a0 = row * 1024 + ((hh ^ fsw(row)) << 4);
#pragma unroll
    for (int t = 0; t < 32; ++t) ka[t] = *(LAS v4u*)(L + ((a0 ^ ((t & 7) << 5)) + (t >> 3) * 256));
  }
  v4u pa[2][8];
  const int tq = (lane & 15) >> 2, tp = lane & 3, tcb = g & 1;
  auto read_pa = [&]() {
#pragma unroll
    for (int mt = 0; mt < 2; ++mt)
#pragma unroll
      for (int ks = 0; ks < 8; ++ks)
#pragma unroll
        for (int t2 = 0; t2 < 2; ++t2) {
          const int row = 16 * ks + 8 * t2 + 4 * hh + tq;
          const int ch = 8 * w + 4 * mt + 2 * tcb + (tp >> 1);
          const int f = (tq << 2) | ((2 * t2 + hh) & 3);
          const int addr = row * 1024 + ((ch ^ f) << 4) + 8 * (tp & 1);
          const s16x4 v = __builtin_amdgcn_ds_read_tr16_b64_v4i16((LAS s16x4*)(L + addr));
          const v2u u = __builtin_bit_cast(v2u, v);
          pa[mt][ks][2 * t2] = u.x; pa[mt][ks][2 * t2 + 1] = u.y;
        }
  };
  {
    int off[4]; v4u a[4];
#pragma unroll
    for (int i = 0; i < 4; ++i) off[i] = OFF_QABS + b * 32768 + (tid + 512 * i) * 16;
    if (w >= 4) read_pa();

    if (w >= 6) trigtp(rs, OFF_Q + (tid - 384) * 1024 + 1020, tag);
    BAR();

    sleep_n(QA_DELAY);
    poll16s<4>(rs, off, a, QA_SLP);

#pragma unroll
    for (int i = 0; i < 4; ++i) {
      const int head = w + 8 * i, ch = lane;
      *(LAS v4u*)(L + QS + ch * 512 + ((head ^ (ch & 7)) << 4)) = a[i];
    }
  }
  BAR();

  auto scr = [&](int off) { return (off >> 9) * 1024 + 512 + (off & 511); };
  constexpr int SC_P = 0, SC_MX = 8192, SC_LS = 8704;
  f32x16 sacc;
  if (w < 4) {
#pragma unroll
    for (int i = 0; i < 16; ++i) sacc[i] = 0.f;
    auto rdq = [&](int t) { const int ch = 2 * t + hh; return *(LAS v4u*)(L + QS + ch * 512 + ((m32 ^ (ch & 7)) << 4)); };
    v4u qb[8];
#pragma unroll
    for (int t = 0; t < 8; ++t) qb[t] = rdq(t);
    SCHED();
#pragma unroll
    for (int t = 0; t < 32; ++t) {
      const v4u cur = qb[t & 7];
      sacc = mfma32(ka[t], cur, sacc);
      if (t + 8 < 32) qb[t & 7] = rdq(t + 8);
      SCHED();
    }
    float mx = sacc[0];
#pragma unroll
    for (int i = 1; i < 16; ++i) mx = fmaxf(mx, sacc[i]);
    mx = x32_max(mx);
    if (hh == 0) *(LAS float*)(L + scr(SC_MX + (w * 32 + m32) * 4)) = mx;

  }
  BAR();

  float M;
  {
    float m0 = *(LAS float*)(L + scr(SC_MX + (0 * 32 + m32) * 4)), m1 = *(LAS float*)(L + scr(SC_MX + (1 * 32 + m32) * 4));
    float m2 = *(LAS float*)(L + scr(SC_MX + (2 * 32 + m32) * 4)), m3 = *(LAS float*)(L + scr(SC_MX + (3 * 32 + m32) * 4));
    M = fmaxf(fmaxf(m0, m1), fmaxf(m2, m3));
  }
  if (w < 4) {
    float pr[16], ls = 0.f;
#pragma unroll
    for (int i = 0; i < 16; ++i) { pr[i] = __builtin_amdgcn_exp2f(sacc[i] - M); ls += pr[i]; }
    ls = x32_sum(ls);
    if (hh == 0) *(LAS float*)(L + scr(SC_LS + (w * 32 + m32) * 4)) = ls;
#pragma unroll
    for (int sp = 0; sp < 2; ++sp) {
      v4u pf;
#pragma unroll
      for (int t = 0; t < 4; ++t) pf[t] = pk2(pr[8 * sp + 2 * t], pr[8 * sp + 2 * t + 1]);
      *(LAS v4u*)(L + scr(SC_P + ((2 * w + sp) * 64 + lane) * 16)) = pf;
    }
    read_pa();
  }
  BAR();

  float lsum;
  {
    float l0 = *(LAS float*)(L + scr(SC_LS + (0 * 32 + m32) * 4)), l1 = *(LAS float*)(L + scr(SC_LS + (1 * 32 + m32) * 4));
    float l2 = *(LAS float*)(L + scr(SC_LS + (2 * 32 + m32) * 4)), l3 = *(LAS float*)(L + scr(SC_LS + (3 * 32 + m32) * 4));
    lsum = (l0 + l1) + (l2 + l3);
  }
  f32x16 oacc[2];
#pragma unroll
  for (int mt = 0; mt < 2; ++mt)
#pragma unroll
    for (int i = 0; i < 16; ++i) oacc[mt][i] = 0.f;
  {
    v4u pf[8];
#pragma unroll
    for (int ks = 0; ks < 8; ++ks) pf[ks] = *(LAS v4u*)(L + scr(SC_P + (ks * 64 + lane) * 16));
    SCHED();
#pragma unroll
    for (int ks = 0; ks < 8; ++ks) {
      oacc[0] = mfma32(pa[0][ks], pf[ks], oacc[0]);
      oacc[1] = mfma32(pa[1][ks], pf[ks], oacc[1]);
    }
  }

  {
    const float inv = 1.f / lsum;
    LAS unsigned char* st = L + QS + w * 4096;
#pragma unroll
    for (int mt = 0; mt < 2; ++mt)
#pragma unroll
      for (int qd = 0; qd < 4; ++qd) {
        v2u o;
        o.x = pk2(oacc[mt][4 * qd] * inv, oacc[mt][4 * qd + 1] * inv);
        o.y = pk2(oacc[mt][4 * qd + 2] * inv, oacc[mt][4 * qd + 3] * inv);
        *(LAS v2u*)(st + m32 * 128 + (((4 * mt + qd) ^ (m32 & 7)) << 4) + 8 * hh) = o;
      }
    LGKM0();
#pragma unroll
    for (int i = 0; i < 4; ++i) {
      const int head = 8 * i + (lane >> 3), ch = lane & 7;
      const v4u v = *(LAS v4u*)(st + head * 128 + ((ch ^ (head & 7)) << 4));
      st16(rs, OFF_O + ((b * 32 + head) * 2 + (w >> 2)) * 16384 + s * 512 + (w & 3) * 128 + ch * 16, v);
    }

    DRAIN();
    BAR();
    if (w == 0) {
      const float lse = M + __log2f(lsum);
      LAS float* sf = (LAS float*)(L + scr(SC_MX + 1024));
      if (hh == 0) sf[m32] = lse;
      LGKM0();
      if (lane < 8) { const v4u v = *(LAS v4u*)(sf + 4 * lane); st16(rs, OFF_STATS + (b * 32 + s) * 128 + lane * 16, san4(v)); }
    }
    BAR();
  }

  constexpr int RED_E = 32768, XE = RED_E + 11520, HS = XE + 128, HID = HS + 5120, OST = HID + 512;
  if (isE) {
    LAS float* redE = (LAS float*)(L + RED_E);
    U8 ug, uu; U16 ud;
    if (w < 5) {
      load_u8<false>(ug, p.wg + (size_t)(128 * w) * 1728 + 16 * e, 1728, lane);
      load_u8<false>(uu, p.wu + (size_t)(128 * w) * 1728 + 16 * e, 1728, lane);
      SCHED();
    } else {
      const int col = 256 * (w - 5) + 128 * (g >> 1) + 8 * n;
      const u16* pd = p.wd + (size_t)(16 * e + 8 * (g & 1)) * 640 + (col < 640 ? col : 0);
#pragma unroll
      for (int i = 0; i < 8; ++i) ud.w[i] = gld16nt(pd + (size_t)i * 640);
      SCHED();
    }
    const int eb = w >> 1, G0 = (w & 1) ? 3 : 0, NG = (w & 1) ? 2 : 3;
    const bool own = g < 2;
    float ga4[3][4], sb4[3][4], cb4[3][4], xx4[3][4];
    {
      u32 mv[3][3][2], xv[3][2];
      for (int spins = 0;; ++spins) {
        asm volatile("" ::: "memory");
        bool ok = true;
#pragma unroll
        for (int k = 0; k < 3; ++k) {
          const int col = 128 * (G0 + (k < NG ? k : 0)) + 8 * n + 4 * (g & 1);
#pragma unroll
          for (int kind = 0; kind < 3; ++kind) {
            const v2u t = __builtin_bit_cast(v2u, __builtin_amdgcn_raw_buffer_load_b64(rs, OFF_MODR + (kind * 10 + (col >> 6)) * 512 + eb * 128 + (col & 63) * 2, 0, AUX_SC1));
            mv[k][kind][0] = t.x; mv[k][kind][1] = t.y;
            ok = ok && t.x != SENT && t.y != SENT;
          }
        }
        if (__all(ok) || spins > SPIN_CAP) break;
        sleep_n(4);
      }
#pragma unroll
      for (int k = 0; k < 3; ++k) {
        const int col = 128 * (G0 + (k < NG ? k : 0)) + 8 * n + 4 * (g & 1);
        const v2u t = gld8(p.x + eb * 640 + col);
        xv[k][0] = t.x; xv[k][1] = t.y;
      }
#pragma unroll
      for (int k = 0; k < 3; ++k)
#pragma unroll
        for (int i = 0; i < 4; ++i) {
          const int d = i >> 1;
          ga4[k][i] = (i & 1) ? bfhi(mv[k][0][d]) : bflo(mv[k][0][d]);
          sb4[k][i] = (i & 1) ? bfhi(mv[k][1][d]) : bflo(mv[k][1][d]);
          cb4[k][i] = 1.f + ((i & 1) ? bfhi(mv[k][2][d]) : bflo(mv[k][2][d]));
          xx4[k][i] = (i & 1) ? bfhi(xv[k][d]) : bflo(xv[k][d]);
        }
    }
    v4u sel;
#pragma unroll
    for (int t = 0; t < 4; ++t) sel[t] = (n == 2 * t ? 0x3F80u : 0u) | (n == 2 * t + 1 ? 0x3F800000u : 0u);
    v4u fg[4], fu[4], fd[8];
    if (w < 5) {
      tr_u8(ug, fg); tr_u8(uu, fu);
#pragma unroll
      for (int q = 0; q < 4; ++q) { PIN4(fg[q]); PIN4(fu[q]); }
    } else {
      tr_u16(ud, fd);
#pragma unroll
      for (int q = 0; q < 8; ++q) PIN4(fd[q]);
    }

    int off[6]; v4u a[6];
#pragma unroll
    for (int k = 0; k < 3; ++k)
#pragma unroll
      for (int hf = 0; hf < 2; ++hf) {
        const int col = 128 * (G0 + (k < NG ? k : 0)) + 8 * n;
        off[2 * k + hf] = OFF_ATTN + ((g + 4 * hf) * 10 + (col >> 6)) * 512 + eb * 128 + (col & 63) * 2;
      }
    if (w >= 6) { const int t2 = tid - 384; trig4p(rs, OFF_CTX + (t2 & 1) * 32768 + (1 + 2 * ((t2 >> 1) & 1)) * 8192 + (t2 >> 2) * 256 + 252); }
    BAR();

    sleep_n(AT_DELAY);
    poll16s<6>(rs, off, a, AT_SLP);

    float x1[3][4], sm = 0.f;
#pragma unroll
    for (int k = 0; k < 3; ++k) {
      f32x4 d = mfma16(sel, a[2 * k], f32x4{0, 0, 0, 0});
      d = mfma16(sel, a[2 * k + 1], d);
#pragma unroll
      for (int i = 0; i < 4; ++i) { x1[k][i] = xx4[k][i] + ga4[k][i] * d[i]; if (k < NG) sm += x1[k][i]; }
    }
    if (!own) sm = 0.f;
    const float cnt = (float)(128 * NG);
    const float mw = wave_sum(sm) / cnt;
    float q2 = 0.f;
#pragma unroll
    for (int k = 0; k < 3; ++k)
#pragma unroll
      for (int i = 0; i < 4; ++i) { const float d = x1[k][i] - mw; if (k < NG) q2 += d * d; }
    if (!own) q2 = 0.f;
    q2 = wave_sum(q2);
    LAS float* xs = (LAS float*)(L + XE);
    if (lane == 0) { xs[2 * w] = mw; xs[2 * w + 1] = q2; }
    BAR();
    {
      const float mo = xs[2 * (w ^ 1)], qo = xs[2 * (w ^ 1) + 1];
      const float co = 640.f - cnt;
      const float m = (mw * cnt + mo * co) * (1.f / 640.f);
      const float dl = mw - mo;
      const float rstd = rsqrtf((q2 + qo + dl * dl * (cnt * co * (1.f / 640.f))) * (1.f / 640.f) + 1e-5f);
#pragma unroll
      for (int k = 0; k < 3; ++k) {
        float hv[4];
#pragma unroll
        for (int i = 0; i < 4; ++i) hv[i] = (x1[k][i] - m) * rstd * cb4[k][i] + sb4[k][i];
        if (own && k < NG) *(LAS v2u*)(L + HS + eb * 1280 + (128 * (G0 + k) + 8 * n + 4 * g) * 2) = v2u{pk2(hv[0], hv[1]), pk2(hv[2], hv[3])};
      }
    }
    BAR();

    if (w < 5) {
      const v4u af = *(LAS v4u*)(L + HS + (n >> 2) * 1280 + (128 * w + 32 * (n & 3) + 8 * g) * 2);
      f32x4 ag[4], au[4];
#pragma unroll
      for (int q = 0; q < 4; ++q) { ag[q] = mfma16(af, fg[q], f32x4{0, 0, 0, 0}); au[q] = mfma16(af, fu[q], f32x4{0, 0, 0, 0}); }
      {
        const int rp = n >> 2;
        float vg[4], vu[4];
#pragma unroll
        for (int q = 0; q < 4; ++q) {
          vg[q] = rp == 0 ? ag[q][0] : rp == 1 ? ag[q][1] : rp == 2 ? ag[q][2] : ag[q][3];
          vu[q] = rp == 0 ? au[q][0] : rp == 1 ? au[q][1] : rp == 2 ? au[q][2] : au[q][3];
        }
        LAS float* dst = redE + (w * 4 + rp) * 144 + g * 16 + 4 * (n & 3);
        *(LAS f32x4*)dst = f32x4{vg[0], vg[1], vg[2], vg[3]};
        *(LAS f32x4*)(dst + 64) = f32x4{vu[0], vu[1], vu[2], vu[3]};
      }
    }
    BAR();

    if (w >= 5) {
      f32x4 gr[5], ur[5];
#pragma unroll
      for (int k = 0; k < 5; ++k) { gr[k] = *(LAS f32x4*)(redE + (4 * k + g) * 144 + 4 * n); ur[k] = *(LAS f32x4*)(redE + (4 * k + g) * 144 + 64 + 4 * n); }
      SCHED();
      const f32x4 gg = (gr[0] + gr[1]) + (gr[2] + gr[3]) + gr[4], uu2 = (ur[0] + ur[1]) + (ur[2] + ur[3]) + ur[4];
      float gs[4], us[4];
#pragma unroll
      for (int c = 0; c < 4; ++c) { gs[c] = x32_sum(x16_sum(gg[c])); us[c] = x32_sum(x16_sum(uu2[c])); }
      const float gv = g == 0 ? gs[0] : g == 1 ? gs[1] : g == 2 ? gs[2] : gs[3];
      const float uv2 = g == 0 ? us[0] : g == 1 ? us[1] : g == 2 ? us[2] : us[3];
      const float y = 0.7978845608028654f * (gv + 0.044715f * gv * gv * gv);
      const float th = 1.f - 2.f * __builtin_amdgcn_rcpf(__expf(2.f * y) + 1.f);
      const float hd = 0.5f * gv * (1.f + th) * uv2;
      LAS unsigned char* hb = L + HID + (w - 5) * 128;
      *(LAS u16*)(hb + n * 8 + g * 2) = (u16)(pk2(hd, 0.f) & 0xFFFFu);
      LGKM0();
      v4u af = *(LAS v4u*)(hb + (n & 3) * 32 + (g & 1) * 16);
      if ((g >> 1) != ((n >> 2) & 1)) af = v4u{0, 0, 0, 0};
      f32x4 acc[8];
#pragma unroll
      for (int q = 0; q < 8; ++q) acc[q] = mfma16(af, fd[q], f32x4{0, 0, 0, 0});
      LAS unsigned char* ob = L + OST + (w - 5) * 2048;
      if (g < 2) {
#pragma unroll
        for (int bb = 0; bb < 4; ++bb) {
          v4u o;
#pragma unroll
          for (int t = 0; t < 4; ++t) o[t] = pk2(acc[2 * t][bb], acc[2 * t + 1][bb]);
          *(LAS v4u*)(ob + (8 * g + (n >> 1)) * 128 + bb * 32 + (n & 1) * 16) = san4(o);
        }
      }
      LGKM0();
#pragma unroll
      for (int i = 0; i < 2; ++i) {
        const int ln = 8 * i + (lane >> 3), grp = 16 * (w - 5) + ln;
        const v4u o = *(LAS v4u*)(ob + ln * 128 + (lane & 7) * 16);
        if (grp < 40) st16(rs, OFF_OUTP + (grp * 108 + e) * 128 + (lane & 7) * 16, o);
      }
    }

  }

}

__global__ void __launch_bounds__(512, 2) mk_kernel(Params p) {
  extern __shared__ __attribute__((aligned(16))) unsigned char smem[];
  LAS unsigned char* L = (LAS unsigned char*)smem;
  const int tid = threadIdx.x, lane = tid & 63;
  const int w = __builtin_amdgcn_readfirstlane(tid >> 6);
  const int x = blockIdx.x & 7, r = blockIdx.x >> 3;
  const rsrc_t rs = __builtin_amdgcn_make_buffer_rsrc((void*)p.ws, 0, WS_USED, 0x00020000);
  if (r >= 21 && w < 3) {
    unsigned long long pc;
    asm volatile("s_getpc_b64 %0" : "=s"(pc));
    const unsigned char* base = (const unsigned char*)(pc & ~255ull);
    const int o0 = ((r - 21) * 3 + w) * 1024 + lane * 16;
    if (o0 + 16 <= CODE_BYTES) { const v4u cpf0 = *(const GAS v4u*)(base + o0); asm volatile("" :: "v"(cpf0)); }
  }
  if (r < 16) ch_role(p, L, rs, x, r, tid, lane, w);
  else att_role(p, L, rs, x, r - 16, tid, lane, w);
}
}

extern "C" void kernel_launch(void* const* d_in, const int* in_sizes, int n_in, void* d_out, int out_size, void* d_ws, size_t ws_size, hipStream_t stream) {
  Params p;
  p.x = (const u16*)d_in[0]; p.ckv = (const u16*)d_in[1]; p.wq = (const u16*)d_in[2]; p.wuk = (const u16*)d_in[3];
  p.wuv = (const u16*)d_in[4]; p.wo = (const u16*)d_in[5]; p.wg = (const u16*)d_in[7]; p.wu = (const u16*)d_in[8];
  p.wd = (const u16*)d_in[9]; p.c = (const u16*)d_in[10]; p.wmod = (const u16*)d_in[11];
  p.out = (u16*)d_out; p.ws = (unsigned char*)d_ws;
  (void)hipFuncSetAttribute((const void*)mk_kernel, hipFuncAttributeMaxDynamicSharedMemorySize, LDS_BYTES);
  hipLaunchKernelGGL(mk_kernel, dim3(256), dim3(512), LDS_BYTES, stream, p);
}
```
